```python
import torch

def _softmax(x, axis=-1):
    return torch.softmax(x, dim=axis)

def _gelu(x):
    return torch.nn.functional.gelu(x, approximate='tanh')

def _silu(x):
    return torch.nn.functional.silu(x)

def _sigmoid(x):
    return torch.sigmoid(x)

def _layernorm(x, axis=-1, eps=1e-05):
    m = x.mean(dim=axis, keepdim=True)
    v = x.var(dim=axis, keepdim=True, unbiased=False)
    return (x - m) / torch.sqrt(v + eps)

def _scatter_add(tgt, idx, src, axis=0):
    return tgt.clone().index_add_(axis, idx, src)

def _segment_sum(src, seg, n):
    out = torch.zeros((n,) + src.shape[1:], dtype=src.dtype)
    return out.index_add_(0, seg, src)

def _pad(x, pw):
    flat = tuple((v for pair in reversed(pw) for v in pair))
    return torch.nn.functional.pad(x, flat)
kernel_name = 'adaLN_cond__linear_kernel_hedgehog__dense_activation_relu__prompt'

def setup_inputs():

    _w = lambda a: (lambda t: t.to(torch.bfloat16) if t.is_floating_point() else t)(torch.as_tensor(a))

    def _A_setup():
        B, S, D, H = (1, 2048, 512, 32)
        Dh = D // H
        return {'x': _w((lambda *s: torch.randn(*s, dtype=torch.bfloat16) * 0.05)(B, S, D)), 'wqkv': _w((lambda *s: torch.randn(*s, dtype=torch.bfloat16) * 0.4)(D, 3 * D)), 'w_phi': _w((lambda *s: torch.randn(*s, dtype=torch.bfloat16) * 0.4)(H, Dh, Dh) * Dh ** (-0.5)), 'wo': _w((lambda *s: torch.randn(*s, dtype=torch.bfloat16) * 0.05)(D, D))}
    _A_in = _A_setup()

    def _B_setup():
        B, S, D, Dff = (1, 2048, 512, 1376)
        return {'x': _w((lambda *s: torch.randn(*s, dtype=torch.bfloat16) * 0.05)(B, S, D)), 'w1': _w((lambda *s: torch.randn(*s, dtype=torch.bfloat16) * 0.05)(D, Dff)), 'w2': _w((lambda *s: torch.randn(*s, dtype=torch.bfloat16) * 0.05)(Dff, D))}
    _B_in = _B_setup()
    return {'A_x': _A_in['x'], 'A_wqkv': _A_in['wqkv'], 'A_w_phi': _A_in['w_phi'], 'A_wo': _A_in['wo'], 'B_x': _B_in['x'], 'B_w1': _B_in['w1'], 'B_w2': _B_in['w2'], 'c': _w((lambda *s: torch.randn(*s, dtype=torch.bfloat16) * 0.1)(1, 512) * 8.0), 'w_mod': _w((lambda *s: torch.randn(*s, dtype=torch.bfloat16) * 0.1)(512, 6 * 512) * 512 ** (-0.5) * 8.0)}

def reference(A_x, A_wqkv, A_w_phi, A_wo, B_x, B_w1, B_w2, c, w_mod):

    def _A_ref(x, wqkv, w_phi, wo):
        B, S, D = x.shape
        H = 32
        Dh = D // H
        qkv = (lambda x, *d: x.permute(*d))((x @ wqkv).reshape(B, S, 3, H, Dh), 2, 0, 3, 1, 4)
        q, k, v = (qkv[0], qkv[1], qkv[2])
        pq = torch.einsum('bhsd,hdf->bhsf', q, w_phi)
        pk = torch.einsum('bhsd,hdf->bhsf', k, w_phi)
        phi_q = torch.concatenate([_softmax(pq, axis=-1), _softmax(-pq, axis=-1)], axis=-1)
        phi_k = torch.concatenate([_softmax(pk, axis=-1), _softmax(-pk, axis=-1)], axis=-1)
        kv = torch.einsum('bhsf,bhsd->bhfd', phi_k, v)
        z = torch.einsum('bhsf->bhf', phi_k)
        num = torch.einsum('bhqf,bhfd->bhqd', phi_q, kv)
        den = torch.einsum('bhqf,bhf->bhq', phi_q, z)[..., None] + 1e-06
        o = num / den
        return (lambda x, *d: x.permute(*d))(o, 0, 2, 1, 3).reshape(B, S, D) @ wo

    def _B_ref(x, w1, w2):
        h = torch.einsum('bsd,df->bsf', x, w1)
        zero = torch.zeros_like(h)
        h = torch.maximum(h, zero)
        return torch.einsum('bsf,fd->bsd', h, w2)
    x = A_x
    mod = (_silu(c) @ w_mod).reshape(c.shape[0], 6, 1, -1)
    sa, ca, ga, sb, cb, gb = (mod[:, 0].clone(), mod[:, 1].clone(), mod[:, 2].clone(), mod[:, 3].clone(), mod[:, 4].clone(), mod[:, 5].clone())
    h = _layernorm(x, axis=-1) * (1 + ca) + sa
    x = x + ga * _A_ref(h, A_wqkv, A_w_phi, A_wo)
    h = _layernorm(x, axis=-1) * (1 + cb) + sb
    x = x + gb * _B_ref(h, B_w1, B_w2)
    return x
```

```cpp
#include <hip/hip_runtime.h>
#include <hip/hip_bf16.h>
#include <stdint.h>
typedef unsigned short u16;
__device__ __forceinline__ float bf2f(u16 v){ return __uint_as_float(((unsigned)v)<<16); }
__device__ __forceinline__ u16 f2bf(float f){ unsigned u=__float_as_uint(f); u += 0x7fffu + ((u>>16)&1u); return (u16)(u>>16); }
constexpr int S=2048, D=512, H=32, DH=16, DFF=1376;

__global__ void k_mod(const u16* c, const u16* wmod, float* mod){
  int n = blockIdx.x*blockDim.x+threadIdx.x; if(n>=6*D) return;
  float acc=0;
  for(int k=0;k<D;k++){ float cv=bf2f(c[k]); float s=cv/(1.f+__expf(-cv)); s=bf2f(f2bf(s)); acc+=s*bf2f(wmod[k*6*D+n]); }
  mod[n]=bf2f(f2bf(acc));
}
__global__ void k_ln(const u16* x, const float* shift, const float* scale, u16* h){
  int r=blockIdx.x; int t=threadIdx.x; __shared__ float red[2][8];
  float v=bf2f(x[r*D+t]); float s=v;
  for(int o=32;o;o>>=1) s+=__shfl_xor(s,o);
  if((t&63)==0) red[0][t>>6]=s; __syncthreads();
  float m=0; for(int i=0;i<8;i++) m+=red[0][i]; m/=D;
  float d=v-m; float q=d*d; for(int o=32;o;o>>=1) q+=__shfl_xor(q,o);
  if((t&63)==0) red[1][t>>6]=q; __syncthreads();
  float var=0; for(int i=0;i<8;i++) var+=red[1][i]; var/=D;
  float y=d*rsqrtf(var+1e-5f);
  h[r*D+t]=f2bf(y*(1.f+scale[t])+shift[t]);
}
template<int MODE>
__global__ void k_gemm(const u16* A,const u16* B,u16* C,int M,int N,int K,const u16* res,const float* g){
  __shared__ float As[32][33], Bs[32][33];
  int tx=threadIdx.x&31, ty=threadIdx.x>>5;
  int m0=blockIdx.y*32,n0=blockIdx.x*32; float acc[4]={0,0,0,0};
  for(int k0=0;k0<K;k0+=32){
    for(int i=ty;i<32;i+=8){ As[i][tx]=bf2f(A[(m0+i)*K+k0+tx]); Bs[i][tx]=bf2f(B[(k0+i)*N+n0+tx]); }
    __syncthreads();
    for(int k=0;k<32;k++){ float b=Bs[k][tx]; for(int j=0;j<4;j++) acc[j]+=As[ty+8*j][k]*b; }
    __syncthreads();
  }
  for(int j=0;j<4;j++){ int m=m0+ty+8*j,n=n0+tx; float v=acc[j];
    if(MODE==1) v=fmaxf(v,0.f);
    if(MODE==2) v=bf2f(res[m*N+n])+g[n]*v;
    C[m*N+n]=f2bf(v); }
}
__global__ void k_phi(const u16* qkv,const u16* wphi,float* phi){
  int id=blockIdx.x*blockDim.x+threadIdx.x; int w=id/(S*H); int s=(id/H)%S; int h=id%H;
  float p[16]; for(int f=0;f<16;f++) p[f]=0;
  for(int d=0;d<16;d++){ float q=bf2f(qkv[s*3*D+w*D+h*16+d]); for(int f=0;f<16;f++) p[f]+=q*bf2f(wphi[h*256+d*16+f]); }
  float mx=-1e30f,mn=1e30f; for(int f=0;f<16;f++){mx=fmaxf(mx,p[f]);mn=fminf(mn,p[f]);}
  float s1=0,s2=0; for(int f=0;f<16;f++){ s1+=__expf(p[f]-mx); s2+=__expf(mn-p[f]); }
  float* o=phi+((size_t)(w*S+s)*H+h)*32;
  for(int f=0;f<16;f++){ o[f]=__expf(p[f]-mx)/s1; o[16+f]=__expf(mn-p[f])/s2; }
}
__global__ void k_kv(const float* phi,const u16* qkv,float* kv){
  int h=blockIdx.x; int t=threadIdx.x; if(t>=32*17) return; int f=t/17,d=t%17; float acc=0;
  for(int s=0;s<S;s++){ float p=phi[((size_t)(S+s)*H+h)*32+f]; float v=d<16?bf2f(qkv[s*3*D+2*D+h*16+d]):1.f; acc+=p*v; }
  kv[h*544+t]=acc;
}
__global__ void k_o(const float* phi,const float* kv,u16* o){
  int id=blockIdx.x*blockDim.x+threadIdx.x; int s=id/D,h=(id%D)/16,d=id%16; float num=0,den=0;
  for(int f=0;f<32;f++){ float p=phi[((size_t)s*H+h)*32+f]; num+=p*kv[h*544+f*17+d]; den+=p*kv[h*544+f*17+16]; }
  o[id]=f2bf(num/(den+1e-6f));
}
__global__ void k_empty(){}
#define NEMPTY 0
extern "C" void kernel_launch(void* const* d_in, const int* in_sizes, int n_in, void* d_out, int out_size, void* d_ws, size_t ws_size, hipStream_t stream){
  const u16* x=(const u16*)d_in[0]; const u16* wqkv=(const u16*)d_in[1]; const u16* wphi=(const u16*)d_in[2]; const u16* wo=(const u16*)d_in[3];
  const u16* w1=(const u16*)d_in[5]; const u16* w2=(const u16*)d_in[6]; const u16* c=(const u16*)d_in[7]; const u16* wmod=(const u16*)d_in[8];
  char* ws=(char*)d_ws; float* mod=(float*)ws; u16* h=(u16*)(ws+(1<<20)); u16* qkv=(u16*)(ws+(4<<20)); float* phi=(float*)(ws+(16<<20));
  float* kv=(float*)(ws+(40<<20)); u16* o=(u16*)(ws+(41<<20)); u16* x1=(u16*)(ws+(44<<20)); u16* hid=(u16*)(ws+(48<<20));
  k_mod<<<12,256,0,stream>>>(c,wmod,mod);
  k_ln<<<S,512,0,stream>>>(x,mod,mod+D,h);
  k_gemm<0><<<dim3(48,64),256,0,stream>>>(h,wqkv,qkv,S,3*D,D,nullptr,nullptr);
  k_phi<<<2*S*H/256,256,0,stream>>>(qkv,wphi,phi);
  k_kv<<<H,576,0,stream>>>(phi,qkv,kv);
  k_o<<<S*D/256,256,0,stream>>>(phi,kv,o);
  k_gemm<2><<<dim3(16,64),256,0,stream>>>(o,wo,x1,S,D,D,x,mod+2*D);
  k_ln<<<S,512,0,stream>>>(x1,mod+3*D,mod+4*D,h);
  k_gemm<1><<<dim3(43,64),256,0,stream>>>(h,w1,hid,S,DFF,D,nullptr,nullptr);
  k_gemm<2><<<dim3(16,64),256,0,stream>>>(hid,w2,(u16*)d_out,S,D,DFF,x1,mod+5*D);
  for(int i=0;i<NEMPTY;i++) k_empty<<<256,256,0,stream>>>();
}
```

```cpp
#include <hip/hip_runtime.h>
#include <hip/hip_bf16.h>
#include <stdint.h>
typedef unsigned short u16;
__device__ __forceinline__ float bf2f(u16 v){ return __uint_as_float(((unsigned)v)<<16); }
__device__ __forceinline__ u16 f2bf(float f){ unsigned u=__float_as_uint(f); u += 0x7fffu + ((u>>16)&1u); return (u16)(u>>16); }
constexpr int S=2048, D=512, H=32, DH=16, DFF=1376;

__global__ void k_mod(const u16* c, const u16* wmod, float* mod){
  int n = blockIdx.x*blockDim.x+threadIdx.x; if(n>=6*D) return;
  float acc=0;
  for(int k=0;k<D;k++){ float cv=bf2f(c[k]); float s=cv/(1.f+__expf(-cv)); s=bf2f(f2bf(s)); acc+=s*bf2f(wmod[k*6*D+n]); }
  mod[n]=bf2f(f2bf(acc));
}
__global__ void k_ln(const u16* x, const float* shift, const float* scale, u16* h){
  int r=blockIdx.x; int t=threadIdx.x; __shared__ float red[2][8];
  float v=bf2f(x[r*D+t]); float s=v;
  for(int o=32;o;o>>=1) s+=__shfl_xor(s,o);
  if((t&63)==0) red[0][t>>6]=s; __syncthreads();
  float m=0; for(int i=0;i<8;i++) m+=red[0][i]; m/=D;
  float d=v-m; float q=d*d; for(int o=32;o;o>>=1) q+=__shfl_xor(q,o);
  if((t&63)==0) red[1][t>>6]=q; __syncthreads();
  float var=0; for(int i=0;i<8;i++) var+=red[1][i]; var/=D;
  float y=d*rsqrtf(var+1e-5f);
  h[r*D+t]=f2bf(y*(1.f+scale[t])+shift[t]);
}
template<int MODE>
__global__ void k_gemm(const u16* A,const u16* B,u16* C,int M,int N,int K,const u16* res,const float* g){
  __shared__ float As[32][33], Bs[32][33];
  int tx=threadIdx.x&31, ty=threadIdx.x>>5;
  int m0=blockIdx.y*32,n0=blockIdx.x*32; float acc[4]={0,0,0,0};
  for(int k0=0;k0<K;k0+=32){
    for(int i=ty;i<32;i+=8){ As[i][tx]=bf2f(A[(m0+i)*K+k0+tx]); Bs[i][tx]=bf2f(B[(k0+i)*N+n0+tx]); }
    __syncthreads();
    for(int k=0;k<32;k++){ float b=Bs[k][tx]; for(int j=0;j<4;j++) acc[j]+=As[ty+8*j][k]*b; }
    __syncthreads();
  }
  for(int j=0;j<4;j++){ int m=m0+ty+8*j,n=n0+tx; float v=acc[j];
    if(MODE==1) v=fmaxf(v,0.f);
    if(MODE==2) v=bf2f(res[m*N+n])+g[n]*v;
    C[m*N+n]=f2bf(v); }
}
__global__ void k_phi(const u16* qkv,const u16* wphi,float* phi){
  int id=blockIdx.x*blockDim.x+threadIdx.x; int w=id/(S*H); int s=(id/H)%S; int h=id%H;
  float p[16]; for(int f=0;f<16;f++) p[f]=0;
  for(int d=0;d<16;d++){ float q=bf2f(qkv[s*3*D+w*D+h*16+d]); for(int f=0;f<16;f++) p[f]+=q*bf2f(wphi[h*256+d*16+f]); }
  float mx=-1e30f,mn=1e30f; for(int f=0;f<16;f++){mx=fmaxf(mx,p[f]);mn=fminf(mn,p[f]);}
  float s1=0,s2=0; for(int f=0;f<16;f++){ s1+=__expf(p[f]-mx); s2+=__expf(mn-p[f]); }
  float* o=phi+((size_t)(w*S+s)*H+h)*32;
  for(int f=0;f<16;f++){ o[f]=__expf(p[f]-mx)/s1; o[16+f]=__expf(mn-p[f])/s2; }
}
__global__ void k_kv(const float* phi,const u16* qkv,float* kv){
  int h=blockIdx.x; int t=threadIdx.x; if(t>=32*17) return; int f=t/17,d=t%17; float acc=0;
  for(int s=0;s<S;s++){ float p=phi[((size_t)(S+s)*H+h)*32+f]; float v=d<16?bf2f(qkv[s*3*D+2*D+h*16+d]):1.f; acc+=p*v; }
  kv[h*544+t]=acc;
}
__global__ void k_o(const float* phi,const float* kv,u16* o){
  int id=blockIdx.x*blockDim.x+threadIdx.x; int s=id/D,h=(id%D)/16,d=id%16; float num=0,den=0;
  for(int f=0;f<32;f++){ float p=phi[((size_t)s*H+h)*32+f]; num+=p*kv[h*544+f*17+d]; den+=p*kv[h*544+f*17+16]; }
  o[id]=f2bf(num/(den+1e-6f));
}
#define PROBE 0
#define STAT 0

#define GAS __attribute__((address_space(1)))
#define LAS __attribute__((address_space(3)))
typedef unsigned v4u __attribute__((ext_vector_type(4)));
__device__ __forceinline__ unsigned long long rt(){ unsigned long long t; asm volatile("s_memrealtime %0\n\ts_waitcnt lgkmcnt(0)":"=s"(t)::"memory"); return t; }
#define R4(x) x x x x
#define R16(x) R4(R4(x))
#define R64(x) R4(R16(x))
#define R256(x) R4(R64(x))
#define R1024(x) R4(R256(x))
#define GL(p,o,aux) __builtin_amdgcn_global_load_lds((const GAS void*)(p),(LAS void*)(smem+(o)+w*1024),16,0,aux)
__global__ __launch_bounds__(256) void k_probe(const char* x,const char* wqkv,const char* wo,const char* w1,const char* w2,const char* wmod,char* scratch,unsigned long long* out){
  __shared__ char smem[98304];
  const int t=threadIdx.x,b=blockIdx.x; const int w=__builtin_amdgcn_readfirstlane(t>>6);
  unsigned long long t0=rt();
  unsigned long long t1=rt();
  if(t==0){ out[b*2]=t0; out[b*2+1]=t1; }
}
__global__ void k_spin(const unsigned long long* out){
  unsigned long long mn=~0ull,mx=0,mxs=0,sum=0;
  for(int b=0;b<256;b++){ unsigned long long a=out[b*2],c=out[b*2+1]; mn=a<mn?a:mn; mxs=a>mxs?a:mxs; mx=c>mx?c:mx; sum+=c-a; }
  unsigned long long v = STAT==0? sum*200/256 : STAT==1? (mxs-mn)*200 : (mx-mxs)*200;
  if(v>1000000) v=1000000;
  unsigned long long t0=rt(); while(rt()-t0<v) __builtin_amdgcn_s_sleep(4);
}
__global__ void k_empty(){}
#define NEMPTY 0
extern "C" void kernel_launch(void* const* d_in, const int* in_sizes, int n_in, void* d_out, int out_size, void* d_ws, size_t ws_size, hipStream_t stream){
  const u16* x=(const u16*)d_in[0]; const u16* wqkv=(const u16*)d_in[1]; const u16* wphi=(const u16*)d_in[2]; const u16* wo=(const u16*)d_in[3];
  const u16* w1=(const u16*)d_in[5]; const u16* w2=(const u16*)d_in[6]; const u16* c=(const u16*)d_in[7]; const u16* wmod=(const u16*)d_in[8];
  char* ws=(char*)d_ws; float* mod=(float*)ws; u16* h=(u16*)(ws+(1<<20)); u16* qkv=(u16*)(ws+(4<<20)); float* phi=(float*)(ws+(16<<20));
  float* kv=(float*)(ws+(40<<20)); u16* o=(u16*)(ws+(41<<20)); u16* x1=(u16*)(ws+(44<<20)); u16* hid=(u16*)(ws+(48<<20));
  k_probe<<<256,256,0,stream>>>((const char*)x,(const char*)wqkv,(const char*)wo,(const char*)w1,(const char*)w2,(const char*)wmod,ws+(100<<20),(unsigned long long*)(ws+(90<<20)));
  k_spin<<<1,1,0,stream>>>((unsigned long long*)(ws+(90<<20)));
  k_mod<<<12,256,0,stream>>>(c,wmod,mod);
  k_ln<<<S,512,0,stream>>>(x,mod,mod+D,h);
  k_gemm<0><<<dim3(48,64),256,0,stream>>>(h,wqkv,qkv,S,3*D,D,nullptr,nullptr);
  k_phi<<<2*S*H/256,256,0,stream>>>(qkv,wphi,phi);
  k_kv<<<H,576,0,stream>>>(phi,qkv,kv);
  k_o<<<S*D/256,256,0,stream>>>(phi,kv,o);
  k_gemm<2><<<dim3(16,64),256,0,stream>>>(o,wo,x1,S,D,D,x,mod+2*D);
  k_ln<<<S,512,0,stream>>>(x1,mod+3*D,mod+4*D,h);
  k_gemm<1><<<dim3(43,64),256,0,stream>>>(h,w1,hid,S,DFF,D,nullptr,nullptr);
  k_gemm<2><<<dim3(16,64),256,0,stream>>>(hid,w2,(u16*)d_out,S,D,DFF,x1,mod+5*D);
  for(int i=0;i<NEMPTY;i++) k_empty<<<256,256,0,stream>>>();
}
```

```cpp
#include <hip/hip_runtime.h>
#include <stdint.h>

struct Srd { const void* p; uint32_t n; uint32_t f; };
struct Args { Srd s[10]; };

__global__ void __launch_bounds__(256, 1) __attribute__((amdgpu_waves_per_eu(1, 1))) fused_block(Args a) {
#if defined(__HIP_DEVICE_COMPILE__)
  __shared__ __attribute__((aligned(16))) char lds[163840];
  asm volatile(
   ".Lfb_start:\n"
   "s_load_dwordx16 s[8:23], s[0:1], 0\n"
   "s_load_dwordx16 s[24:39], s[0:1], 64\n"
   "s_load_dwordx8 s[40:47], s[0:1], 0x80\n"
   "s_memrealtime s[68:69]\n"
   "s_getpc_b64 s[4:5]\n"
   ".Lfb_pc:\n"
   "s_mov_b32 s6, .Lfb_end-.Lfb_pc\n"
   "s_mov_b32 s7, 0x20000\n"
   "s_lshr_b32 s52, s2, 3\n"
   "s_lshl_b32 s56, s52, 7\n"
   "v_lshlrev_b32_e64 v1, 12, v0\n"
   "buffer_load_dword v5, v1, s[4:7], s56 offen\n"
   "v_lshrrev_b32_e64 v2, 6, v0\n"
   "v_and_b32 v6, 63, v0\n"
   "v_readfirstlane_b32 s51, v2\n"
   "v_lshlrev_b32 v9, 4, v6\n"
   "s_and_b32 s53, s2, 7\n"
   "s_lshr_b32 s56, s52, 3\n"
   "s_lshl2_add_u32 s54, s53, s56\n"
   "s_and_b32 s55, s52, 7\n"
   "s_lshr_b32 s66, s52, 2\n"
   "s_and_b32 s57, s52, 3\n"
   "s_lshl2_add_u32 s67, s53, s57\n"
   "s_mul_i32 s71, s66, 0x60000\n"
   "s_lshl_b32 s70, s54, 16\n"
   "v_mov_b32 v4, 0\n"
   "v_lshlrev_b32_e64 v1, 1, v6\n"
   "s_lshl_b32 s58, s66, 7\n"
   "s_cmp_eq_u32 s51, 0\n"
   "s_cbranch_scc0 .Lfb_skip1\n"
   "v_lshrrev_b32_e64 v2, 2, v6\n"
   "v_mul_u32_u24 v2, 0x6000, v2\n"
   "v_and_b32_e64 v3, 48, v9\n"
   "v_or_b32 v2, v2, v3\n"
   "s_lshl_b32 s56, s67, 6\n"
   "s_add_u32 s56, s56, s71\n"
   "s_waitcnt lgkmcnt(0)\n"
   "buffer_load_dwordx2 v[10:11], v4, s[44:47], 0 offen sc1\n"
   "buffer_load_ushort v12, v1, s[32:35], s58 offen\n"
   "buffer_load_dwordx4 v[16:19], v2, s[36:39], s56 offen\n"
   "s_add_u32 s56, s56, 0x1800\n"
   "buffer_load_dwordx4 v[20:23], v2, s[36:39], s56 offen\n"
   "s_add_u32 s56, s56, 0x1800\n"
   "buffer_load_dwordx4 v[24:27], v2, s[36:39], s56 offen\n"
   "s_add_u32 s56, s56, 0x1800\n"
   "buffer_load_dwordx4 v[28:31], v2, s[36:39], s56 offen\n"
   ".Lfb_skip1:\n"
   "s_cmp_ge_u32 s51, 2\n"
   "s_cbranch_scc0 .Lfb_skip2\n"
   "s_lshl1_add_u32 s57, s55, s51\n"
   "s_sub_u32 s57, s57, 2\n"
   "s_lshl_b32 s57, s57, 12\n"
   "s_add_u32 s57, s57, s70\n"
   "s_waitcnt lgkmcnt(0)\n"
   "buffer_load_dwordx2 v[10:11], v4, s[44:47], 0 offen sc1\n"
   "buffer_load_ushort v12, v1, s[32:35], s58 offen\n"
   "s_nop 15\n"
   "s_nop 15\n"
   "s_nop 15\n"
   "buffer_load_dwordx4 v[16:19], v9, s[8:11], s57 offen\n"
   "buffer_load_dwordx4 v[20:23], v9, s[8:11], s57 offen offset:1024\n"
   "buffer_load_dwordx4 v[24:27], v9, s[8:11], s57 offen offset:2048\n"
   "buffer_load_dwordx4 v[28:31], v9, s[8:11], s57 offen offset:3072\n"
   ".Lfb_skip2:\n"
   "s_cmp_eq_u32 s51, 1\n"
   "s_cbranch_scc0 .Lfb_skip3\n"
   "v_lshrrev_b32_e64 v2, 3, v6\n"
   "v_and_b32 v3, 0x70, v9\n"
   "v_lshl_or_b32 v2, v2, 10, v3\n"
   "s_lshl_b32 s56, s2, 7\n"
   "s_add_u32 s56, s56, 0x1000\n"
   "s_cmp_lt_u32 s2, 8\n"
   "s_cselect_b32 s56, s56, 0x40000000\n"
   "s_waitcnt lgkmcnt(0)\n"
   "v_mov_b32 v32, s68\n"
   "v_mov_b32 v33, s68\n"
   "v_mov_b32 v34, s68\n"
   "v_mov_b32 v35, s68\n"
   "buffer_store_dwordx4 v[32:35], v2, s[44:47], s56 offen sc1\n"
   "buffer_load_dwordx2 v[10:11], v4, s[44:47], 0 offen sc1\n"
   "buffer_load_ushort v12, v1, s[32:35], s58 offen\n"
   ".Lfb_skip3:\n"
   "s_waitcnt lgkmcnt(0)\n"
   "s_mov_b32 s0, s68\n"
   "s_lshl_b32 s61, s51, 12\n"
   "s_lshl2_add_u32 s1, s55, s51\n"
   "s_lshl_b32 s58, s1, 9\n"
   "s_add_u32 m0, s61, 0x400\n"
   "s_mov_b32 exec_lo, 0xffffffff\n"
   "s_mov_b32 exec_hi, 0\n"
   "buffer_load_dwordx4 v9, s[16:19], s58 offen lds\n"
   "s_mov_b64 exec, -1\n"
   "v_and_b32_e64 v7, 15, v0\n"
   "v_bfe_u32 v8, v0, 4, 2\n"
   "v_lshlrev_b32 v2, 12, v8\n"
   "v_lshl_or_b32 v2, v7, 4, v2\n"
   "s_add_u32 s3, s51, s53\n"
   "s_and_b32 s3, s3, 3\n"
   "s_lshl_b32 s56, s3, 8\n"
   "v_add_u32 v13, s56, v2\n"
   "s_mov_b32 s50, 0\n"
   "s_mov_b32 s62, 0x3fb8aa3b\n"
   "s_mov_b32 s63, 0x3fb8aa3b\n"
   "s_mov_b32 s64, 0x3f803f80\n"
   "s_mov_b32 s65, 0xffff0000\n"
   "v_mov_b32_e64 v14, 0x40000000\n"
   "v_mov_b32 v15, 0x3ff\n"
   "s_lshl3_add_u32 s72, s54, s55\n"
   "s_lshl_b32 s72, s72, 7\n"
   "s_mul_i32 s56, s66, 0x3000\n"
   "s_add_u32 s56, s56, 0x20000\n"
   "s_lshl_b32 s67, s67, 7\n"
   "s_add_u32 s73, s56, s67\n"
   "s_mul_i32 s56, s51, 0x18000\n"
   "s_add_u32 s56, s56, s71\n"
   "s_add_u32 s56, s56, s67\n"
   "s_add_u32 s74, s56, 0x800\n"
   "v_lshlrev_b32 v160, 3, v6\n"
   "v_lshrrev_b32 v165, 3, v6\n"
   "v_bfe_u32 v2, v6, 1, 2\n"
   "s_and_b32 s56, s51, 1\n"
   "s_lshl_b32 s56, s56, 1\n"
   "v_and_or_b32 v3, v8, 1, s56\n"
   "v_xor_b32 v2, v2, v3\n"
   "v_and_b32 v3, 16, v9\n"
   "v_lshl_or_b32 v2, v2, 5, v3\n"
   "v_mul_u32_u24 v1, 0xc00, v165\n"
   "v_add_u32 v161, v1, v2\n"
   "v_lshrrev_b32 v163, 2, v7\n"
   "v_bfe_u32 v162, v6, 3, 2\n"
   "s_mul_i32 s80, s51, 0xc00\n"
   "s_lshl_b32 s79, s55, 7\n"
   "s_lshl3_add_u32 s56, s80, s79\n"
   "s_add_u32 s57, s53, 0\n"
   "s_and_b32 s57, s57, 3\n"
   "s_mul_i32 s57, s57, 0x60000\n"
   "s_add_u32 s75, s56, s57\n"
   "s_add_u32 s57, s53, 1\n"
   "s_and_b32 s57, s57, 3\n"
   "s_mul_i32 s57, s57, 0x60000\n"
   "s_add_u32 s76, s56, s57\n"
   "s_add_u32 s57, s53, 2\n"
   "s_and_b32 s57, s57, 3\n"
   "s_mul_i32 s57, s57, 0x60000\n"
   "s_add_u32 s77, s56, s57\n"
   "s_add_u32 s59, s70, 0\n"
   "buffer_load_dwordx4 v[36:39], v13, s[8:11], s59 offen\n"
   "s_add_u32 s57, s53, 3\n"
   "s_and_b32 s57, s57, 3\n"
   "s_mul_i32 s57, s57, 0x60000\n"
   "s_add_u32 s78, s56, s57\n"
   "v_and_b32 v164, 24, v160\n"
   "v_mul_u32_u24 v1, 0xc00, v8\n"
   "v_lshl_add_u32 v1, v163, 7, v1\n"
   "v_xor_b32_e64 v2, s51, v162\n"
   "v_lshl_or_b32 v2, v2, 5, v164\n"
   "v_add_u32_e64 v1, v1, v2\n"
   "v_add_u32 v166, 0x10000, v1\n"
   "v_add_u32 v167, 0x1c000, v1\n"
   "v_and_b32_e64 v1, 7, v7\n"
   "buffer_load_dwordx4 v[44:47], v13, s[8:11], s59 offen offset:1024\n"
   "v_lshlrev_b32 v2, 2, v8\n"
   "v_xor_b32 v1, v1, v2\n"
   "v_lshlrev_b32_e64 v1, 4, v1\n"
   "v_lshl_or_b32 v1, v7, 8, v1\n"
   "v_add_u32 v168, s61, v1\n"
   "v_xor_b32 v169, 16, v168\n"
   "v_xor_b32 v170, 32, v168\n"
   "v_xor_b32 v171, 48, v168\n"
   "v_xor_b32 v1, v7, v8\n"
   "v_lshlrev_b32 v1, 4, v1\n"
   "v_lshl_or_b32 v172, v8, 8, v1\n"
   "v_xor_b32 v173, 64, v172\n"
   "v_add_u32 v174, s61, v160\n"
   "buffer_load_dwordx4 v[52:55], v13, s[8:11], s59 offen offset:2048\n"
   "v_lshl_add_u32 v175, v7, 5, s61\n"
   "v_lshl_add_u32 v176, v8, 5, s61\n"
   "v_add_u32 v177, s61, v9\n"
   "v_lshrrev_b32 v182, 5, v6\n"
   "v_and_b32 v183, 0x1f0, v9\n"
   "v_lshl_or_b32 v178, v182, 11, v183\n"
   "v_cvt_f32_u32 v180, v182\n"
   "v_lshrrev_b32 v1, 2, v6\n"
   "v_and_b32_e64 v2, 48, v9\n"
   "v_lshl_or_b32 v179, v1, 7, v2\n"
   "s_lshl_b32 s81, s3, 9\n"
   "v_and_b32 v1, 3, v6\n"
   "v_mul_u32_u24 v1, 0xc00, v1\n"
   "buffer_load_dwordx4 v[60:63], v13, s[8:11], s59 offen offset:3072\n"
   "v_lshl_add_u32 v1, v163, 10, v1\n"
   "v_cmp_gt_u32 vcc, 12, v6\n"
   "s_lshl2_add_u32 s56, s52, s51\n"
   "s_mul_i32 s56, s56, 0x3000\n"
   "v_cndmask_b32 v184, v14, v1, vcc\n"
   "s_sub_u32 s57, 0x180, s12\n"
   "s_and_b32 s57, s57, 0x380\n"
   "s_add_u32 s82, s56, s57\n"
   "v_lshl_add_u32 v1, v6, 2, s51\n"
   "v_lshlrev_b32_e64 v2, 7, v1\n"
   "s_mul_i32 s56, s52, 43\n"
   "v_add_lshl_u32 v3, v1, s56, 10\n"
   "v_cmp_gt_u32 vcc, 43, v1\n"
   "s_add_u32 s59, s70, 0x4000\n"
   "buffer_load_dwordx4 v[68:71], v13, s[8:11], s59 offen\n"
   "s_nop 0\n"
   "v_cndmask_b32 v185, v14, v2, vcc\n"
   "v_cndmask_b32 v187, v14, v3, vcc\n"
   "s_lshl_b32 s56, s52, 4\n"
   "v_add_lshl_u32 v3, v1, s56, 10\n"
   "v_cmp_gt_u32 vcc, 16, v1\n"
   "s_nop 0\n"
   "s_nop 0\n"
   "v_cndmask_b32 v186, v14, v2, vcc\n"
   "v_cndmask_b32 v188, v14, v3, vcc\n"
   "s_mul_i32 s56, s55, 12\n"
   "s_mul_i32 s57, s55, 10\n"
   "s_add_u32 s57, s57, 6\n"
   "s_min_u32 s84, s56, s57\n"
   "s_add_u32 s56, s56, 12\n"
   "s_add_u32 s57, s57, 10\n"
   "s_min_u32 s85, s56, s57\n"
   "s_sub_u32 s85, s85, s84\n"
   "s_mul_i32 s83, s51, 0x5600\n"
   "s_lshl_b32 s56, s84, 5\n"
   "s_add_u32 s83, s83, s56\n"
   "v_add_u32 v1, 0, v6\n"
   "v_mul_u32_u24 v2, 0xab, v1\n"
   "v_lshrrev_b32 v2, 12, v2\n"
   "v_mul_u32_u24 v3, 24, v2\n"
   "v_sub_u32 v1, v1, v3\n"
   "v_and_b32 v3, 6, v2\n"
   "v_xor_b32_e64 v1, v1, v3\n"
   "v_lshrrev_b32 v3, 1, v1\n"
   "v_cmp_gt_u32 vcc, s85, v3\n"
   "v_mul_u32_u24 v2, 0xac0, v2\n"
   "v_lshl_add_u32 v1, v1, 4, v2\n"
   "v_cndmask_b32 v189, v14, v1, vcc\n"
   "v_add_u32 v1, 64, v6\n"
   "v_mul_u32_u24 v2, 0xab, v1\n"
   "v_lshrrev_b32 v2, 12, v2\n"
   "v_mul_u32_u24 v3, 24, v2\n"
   "v_sub_u32 v1, v1, v3\n"
   "v_and_b32 v3, 6, v2\n"
   "v_xor_b32_e64 v1, v1, v3\n"
   "v_lshrrev_b32 v3, 1, v1\n"
   "v_cmp_gt_u32 vcc, s85, v3\n"
   "v_mul_u32_u24 v2, 0xac0, v2\n"
   "v_lshl_add_u32 v1, v1, 4, v2\n"
   "v_subrev_u32 v1, 0x400, v1\n"
   "v_cndmask_b32 v190, v14, v1, vcc\n"
   "v_add_u32 v1, 0x80, v6\n"
   "v_mul_u32_u24 v2, 0xab, v1\n"
   "v_lshrrev_b32 v2, 12, v2\n"
   "v_mul_u32_u24 v3, 24, v2\n"
   "v_sub_u32 v1, v1, v3\n"
   "v_and_b32 v3, 6, v2\n"
   "v_xor_b32 v1, v1, v3\n"
   "v_lshrrev_b32_e64 v3, 1, v1\n"
   "v_cmp_gt_u32 vcc, s85, v3\n"
   "v_mul_u32_u24 v2, 0xac0, v2\n"
   "v_lshl_add_u32 v1, v1, 4, v2\n"
   "v_subrev_u32 v1, 0x800, v1\n"
   "v_cndmask_b32 v191, v14, v1, vcc\n"
   "v_and_b32 v2, 0x70, v9\n"
   "v_lshl_or_b32 v1, v165, 12, v2\n"
   "v_accvgpr_write_b32 a222, v1\n"
   "v_mul_u32_u24 v1, 0x3000, v165\n"
   "v_or_b32_e64 v1, v1, v2\n"
   "v_accvgpr_write_b32 a228, v1\n"
   "v_lshl_add_u32 v1, v182, 4, v7\n"
   "v_bfe_u32 v2, v7, 1, 2\n"
   "v_and_b32 v3, 2, v162\n"
   "v_xor_b32 v2, v2, v3\n"
   "v_lshlrev_b32_e64 v2, 4, v2\n"
   "v_lshl_or_b32 v1, v1, 6, v2\n"
   "s_add_u32 s56, s61, 0x11000\n"
   "v_add_u32_e64 v1, s56, v1\n"
   "v_accvgpr_write_b32 a220, v1\n"
   "v_lshl_add_u32 v3, v8, 2, v163\n"
   "v_lshlrev_b32 v2, 4, v162\n"
   "v_xor_b32 v2, v2, v164\n"
   "v_lshl_or_b32 v1, v3, 6, v2\n"
   "v_add_u32_e64 v1, s56, v1\n"
   "v_accvgpr_write_b32 a221, v1\n"
   "v_mul_u32_u24 v3, 0x180, v3\n"
   "v_add_u32_e64 v3, v3, v164\n"
   "v_add_u32 v3, 0x10000, v3\n"
   "s_mul_i32 s56, s51, 3\n"
   "v_xor_b32 v2, s56, v162\n"
   "v_lshl_add_u32 v1, v2, 5, v3\n"
   "v_accvgpr_write_b32 a229, v1\n"
   "s_add_u32 s56, s56, 1\n"
   "v_xor_b32 v2, s56, v162\n"
   "v_lshl_add_u32 v1, v2, 5, v3\n"
   "v_accvgpr_write_b32 a230, v1\n"
   "s_add_u32 s56, s56, 1\n"
   "v_xor_b32 v2, s56, v162\n"
   "v_lshl_add_u32 v1, v2, 5, v3\n"
   "v_accvgpr_write_b32 a231, v1\n"
   "v_cmp_gt_u32 vcc, 2, v7\n"
   "v_lshlrev_b32 v1, 6, v7\n"
   "v_lshl_or_b32 v1, v8, 4, v1\n"
   "v_cndmask_b32 v1, v14, v1, vcc\n"
   "v_accvgpr_write_b32 a223, v1\n"
   "v_bfe_u32 v2, v6, 1, 2\n"
   "v_xor_b32 v2, v2, v8\n"
   "v_and_b32_e64 v3, 16, v9\n"
   "v_lshl_or_b32 v2, v2, 5, v3\n"
   "v_lshl_or_b32 v1, v165, 10, v2\n"
   "v_accvgpr_write_b32 a224, v1\n"
   "v_mul_u32_u24 v1, 0x1800, v165\n"
   "v_or_b32_e64 v1, v1, v2\n"
   "v_accvgpr_write_b32 a225, v1\n"
   "v_and_b32_e64 v3, 1, v8\n"
   "v_lshl_or_b32 v1, v3, 1, v182\n"
   "v_lshlrev_b32_e64 v2, 4, v7\n"
   "v_lshl_or_b32 v2, v1, 9, v2\n"
   "v_accvgpr_write_b32 a226, v2\n"
   "v_lshlrev_b32_e64 v2, 5, v1\n"
   "v_accvgpr_write_b32 a227, v2\n"
   "v_lshlrev_b32_e64 v1, 4, v1\n"
   "v_lshl_or_b32 v1, v7, 10, v1\n"
   "v_accvgpr_write_b32 a233, v1\n"
   "v_lshl_or_b32 v1, v182, 2, v3\n"
   "v_lshl_add_u32 v1, v1, 2, v163\n"
   "v_lshl_or_b32 v2, v162, 5, v164\n"
   "v_lshl_or_b32 v1, v1, 7, v2\n"
   "v_accvgpr_write_b32 a232, v1\n"
   "v_mov_b32_e64 v1, s64\n"
   "v_accvgpr_write_b32 a208, v1\n"
   "v_accvgpr_write_b32 a209, v1\n"
   "v_accvgpr_write_b32 a210, v1\n"
   "v_accvgpr_write_b32 a211, v1\n"
   "s_cmp_eq_u32 s51, 0\n"
   "s_cbranch_scc0 .Lfb_skip4\n"
   "s_waitcnt vmcnt(6)\n"
   "v_lshlrev_b32_e64 v4, 16, v12\n"
   "v_mul_f32 v1, 0xbfb8aa3b, v4\n"
   "v_exp_f32 v1, v1\n"
   "s_nop 0\n"
   "v_add_f32 v1, 1.0, v1\n"
   "v_rcp_f32 v1, v1\n"
   "s_nop 0\n"
   "v_mul_f32 v1, v4, v1\n"
   "v_cvt_pk_bf16_f32 v3, v1, 0\n"
   "v_lshlrev_b32 v4, 16, v3\n"
   "v_lshlrev_b32 v1, 2, v164\n"
   "v_lshl_or_b32 v1, v8, 3, v1\n"
   "v_mov_b32_dpp v192, v3 quad_perm:[0,0,0,0] row_mask:0xf bank_mask:0xf\n"
   "v_mov_b32_dpp v193, v4 quad_perm:[0,0,0,0] row_mask:0xf bank_mask:0xf\n"
   "v_mov_b32_dpp v194, v3 quad_perm:[1,1,1,1] row_mask:0xf bank_mask:0xf\n"
   "v_mov_b32_dpp v195, v4 quad_perm:[1,1,1,1] row_mask:0xf bank_mask:0xf\n"
   "v_mov_b32_dpp v196, v3 quad_perm:[2,2,2,2] row_mask:0xf bank_mask:0xf\n"
   "v_mov_b32_dpp v197, v4 quad_perm:[2,2,2,2] row_mask:0xf bank_mask:0xf\n"
   "v_mov_b32_dpp v198, v3 quad_perm:[3,3,3,3] row_mask:0xf bank_mask:0xf\n"
   "v_mov_b32_dpp v199, v4 quad_perm:[3,3,3,3] row_mask:0xf bank_mask:0xf\n"
   "v_dot2_f32_bf16 v200, v16, v192, 0\n"
   "v_dot2_f32_bf16 v201, v16, v193, 0\n"
   "v_dot2_f32_bf16 v202, v17, v192, 0\n"
   "v_dot2_f32_bf16 v203, v17, v193, 0\n"
   "v_dot2_f32_bf16 v204, v18, v192, 0\n"
   "v_dot2_f32_bf16 v205, v18, v193, 0\n"
   "v_dot2_f32_bf16 v206, v19, v192, 0\n"
   "v_dot2_f32_bf16 v207, v19, v193, 0\n"
   "v_dot2_f32_bf16 v200, v20, v194, v200\n"
   "v_dot2_f32_bf16 v201, v20, v195, v201\n"
   "v_dot2_f32_bf16 v202, v21, v194, v202\n"
   "v_dot2_f32_bf16 v203, v21, v195, v203\n"
   "v_dot2_f32_bf16 v204, v22, v194, v204\n"
   "v_dot2_f32_bf16 v205, v22, v195, v205\n"
   "v_dot2_f32_bf16 v206, v23, v194, v206\n"
   "v_dot2_f32_bf16 v207, v23, v195, v207\n"
   "v_dot2_f32_bf16 v200, v24, v196, v200\n"
   "v_dot2_f32_bf16 v201, v24, v197, v201\n"
   "v_dot2_f32_bf16 v202, v25, v196, v202\n"
   "v_dot2_f32_bf16 v203, v25, v197, v203\n"
   "v_dot2_f32_bf16 v204, v26, v196, v204\n"
   "v_dot2_f32_bf16 v205, v26, v197, v205\n"
   "v_dot2_f32_bf16 v206, v27, v196, v206\n"
   "v_dot2_f32_bf16 v207, v27, v197, v207\n"
   "v_dot2_f32_bf16 v200, v28, v198, v200\n"
   "v_dot2_f32_bf16 v201, v28, v199, v201\n"
   "v_dot2_f32_bf16 v202, v29, v198, v202\n"
   "v_dot2_f32_bf16 v203, v29, v199, v203\n"
   "v_dot2_f32_bf16 v204, v30, v198, v204\n"
   "v_dot2_f32_bf16 v205, v30, v199, v205\n"
   "v_dot2_f32_bf16 v206, v31, v198, v206\n"
   "v_dot2_f32_bf16 v207, v31, v199, v207\n"
   "v_readfirstlane_b32 s48, v10\n"
   "v_readfirstlane_b32 s49, v11\n"
   "s_add_u32 s48, s48, 0x7f4a7d95\n"
   "s_addc_u32 s49, s49, 0x9e3779b9\n"
   "s_and_b32 s86, s48, 0x3ff\n"
   "v_permlane32_swap_b32 v200, v204\n"
   "v_permlane32_swap_b32 v201, v205\n"
   "v_permlane32_swap_b32 v202, v206\n"
   "v_permlane32_swap_b32 v203, v207\n"
   "v_add_f32 v200, v200, v204\n"
   "v_add_f32 v202, v202, v206\n"
   "v_add_f32 v201, v201, v205\n"
   "v_add_f32 v203, v203, v207\n"
   "v_permlane16_swap_b32 v200, v202\n"
   "s_nop 0\n"
   "v_permlane16_swap_b32 v201, v203\n"
   "v_add_f32 v200, v200, v202\n"
   "v_add_f32 v201, v201, v203\n"
   "s_nop 0\n"
   "v_add_f32_dpp v200, v200, v200 row_ror:8 row_mask:0xf bank_mask:0xf\n"
   "v_add_f32_dpp v201, v201, v201 row_ror:8 row_mask:0xf bank_mask:0xf\n"
   "s_nop 0\n"
   "v_add_f32_dpp v208, v200, v200 row_ror:4 row_mask:0xf bank_mask:0xf\n"
   "v_add_f32_dpp v209, v201, v201 row_ror:4 row_mask:0xf bank_mask:0xf\n"
   "v_bfi_b32 v208, v15, s86, v208\n"
   "s_mov_b32 exec_lo, 0xf000f\n"
   "s_mov_b32 exec_hi, 0xf000f\n"
   "buffer_store_dwordx2 v[208:209], v1, s[44:47], s73 offen sc1\n"
   "s_mov_b64 exec, -1\n"
   "buffer_load_dwordx4 v[76:79], v13, s[8:11], s59 offen offset:1024\n"
   "buffer_load_dwordx4 v[84:87], v13, s[8:11], s59 offen offset:2048\n"
   "buffer_load_dwordx4 v[92:95], v13, s[8:11], s59 offen offset:3072\n"
   "s_add_u32 s59, s70, 0x8000\n"
   "buffer_load_dwordx4 v[100:103], v13, s[8:11], s59 offen\n"
   "buffer_load_dwordx4 v[108:111], v13, s[8:11], s59 offen offset:1024\n"
   "buffer_load_dwordx4 v[116:119], v13, s[8:11], s59 offen offset:2048\n"
   "buffer_load_dwordx4 v[124:127], v13, s[8:11], s59 offen offset:3072\n"
   "s_add_u32 s59, s70, 0xc000\n"
   "buffer_load_dwordx4 v[132:135], v13, s[8:11], s59 offen\n"
   "buffer_load_dwordx4 v[140:143], v13, s[8:11], s59 offen offset:1024\n"
   ".Lfb_skip4:\n"
   "s_cmp_ge_u32 s51, 2\n"
   "s_cbranch_scc0 .Lfb_skip5\n"
   "buffer_load_dwordx4 v[76:79], v13, s[8:11], s59 offen offset:1024\n"
   "buffer_load_dwordx4 v[84:87], v13, s[8:11], s59 offen offset:2048\n"
   "buffer_load_dwordx4 v[92:95], v13, s[8:11], s59 offen offset:3072\n"
   "s_waitcnt vmcnt(9)\n"
   "v_dot2_f32_bf16 v200, v16, s64, 0\n"
   "v_dot2_f32_bf16 v201, v16, v16, 0\n"
   "v_dot2_f32_bf16 v202, v20, s64, 0\n"
   "v_dot2_f32_bf16 v203, v20, v20, 0\n"
   "v_dot2_f32_bf16 v204, v24, s64, 0\n"
   "v_dot2_f32_bf16 v205, v24, v24, 0\n"
   "v_dot2_f32_bf16 v206, v28, s64, 0\n"
   "v_dot2_f32_bf16 v207, v28, v28, 0\n"
   "v_dot2_f32_bf16 v200, v17, s64, v200\n"
   "v_dot2_f32_bf16 v201, v17, v17, v201\n"
   "v_dot2_f32_bf16 v202, v21, s64, v202\n"
   "v_dot2_f32_bf16 v203, v21, v21, v203\n"
   "v_dot2_f32_bf16 v204, v25, s64, v204\n"
   "v_dot2_f32_bf16 v205, v25, v25, v205\n"
   "v_dot2_f32_bf16 v206, v29, s64, v206\n"
   "v_dot2_f32_bf16 v207, v29, v29, v207\n"
   "v_dot2_f32_bf16 v200, v18, s64, v200\n"
   "v_dot2_f32_bf16 v201, v18, v18, v201\n"
   "v_dot2_f32_bf16 v202, v22, s64, v202\n"
   "v_dot2_f32_bf16 v203, v22, v22, v203\n"
   "s_add_u32 s59, s70, 0x8000\n"
   "buffer_load_dwordx4 v[100:103], v13, s[8:11], s59 offen\n"
   "buffer_load_dwordx4 v[108:111], v13, s[8:11], s59 offen offset:1024\n"
   "buffer_load_dwordx4 v[116:119], v13, s[8:11], s59 offen offset:2048\n"
   "v_dot2_f32_bf16 v204, v26, s64, v204\n"
   "v_dot2_f32_bf16 v205, v26, v26, v205\n"
   "v_dot2_f32_bf16 v206, v30, s64, v206\n"
   "v_dot2_f32_bf16 v207, v30, v30, v207\n"
   "v_dot2_f32_bf16 v200, v19, s64, v200\n"
   "v_dot2_f32_bf16 v201, v19, v19, v201\n"
   "v_dot2_f32_bf16 v202, v23, s64, v202\n"
   "v_dot2_f32_bf16 v203, v23, v23, v203\n"
   "v_dot2_f32_bf16 v204, v27, s64, v204\n"
   "v_dot2_f32_bf16 v205, v27, v27, v205\n"
   "v_dot2_f32_bf16 v206, v31, s64, v206\n"
   "v_dot2_f32_bf16 v207, v31, v31, v207\n"
   "v_readfirstlane_b32 s48, v10\n"
   "v_readfirstlane_b32 s49, v11\n"
   "s_add_u32 s48, s48, 0x7f4a7d95\n"
   "s_addc_u32 s49, s49, 0x9e3779b9\n"
   "s_and_b32 s86, s48, 0x3ff\n"
   "v_permlane32_swap_b32 v200, v204\n"
   "v_permlane32_swap_b32 v201, v205\n"
   "v_permlane32_swap_b32 v202, v206\n"
   "buffer_load_dwordx4 v[124:127], v13, s[8:11], s59 offen offset:3072\n"
   "s_add_u32 s59, s70, 0xc000\n"
   "buffer_load_dwordx4 v[132:135], v13, s[8:11], s59 offen\n"
   "buffer_load_dwordx4 v[140:143], v13, s[8:11], s59 offen offset:1024\n"
   "v_permlane32_swap_b32 v203, v207\n"
   "v_add_f32 v200, v200, v204\n"
   "v_add_f32 v202, v202, v206\n"
   "v_add_f32 v201, v201, v205\n"
   "v_add_f32 v203, v203, v207\n"
   "v_permlane16_swap_b32 v200, v202\n"
   "s_nop 0\n"
   "v_permlane16_swap_b32 v201, v203\n"
   "v_add_f32 v200, v200, v202\n"
   "v_add_f32 v201, v201, v203\n"
   "s_nop 0\n"
   "v_add_f32_dpp v200, v200, v200 row_ror:8 row_mask:0xf bank_mask:0xf\n"
   "v_add_f32_dpp v201, v201, v201 row_ror:8 row_mask:0xf bank_mask:0xf\n"
   "s_nop 0\n"
   "v_add_f32_dpp v200, v200, v200 row_ror:4 row_mask:0xf bank_mask:0xf\n"
   "v_add_f32_dpp v201, v201, v201 row_ror:4 row_mask:0xf bank_mask:0xf\n"
   "s_nop 0\n"
   "v_add_f32_dpp v200, v200, v200 row_ror:2 row_mask:0xf bank_mask:0xf\n"
   "v_add_f32_dpp v201, v201, v201 row_ror:2 row_mask:0xf bank_mask:0xf\n"
   "s_nop 0\n"
   "v_add_f32_dpp v208, v200, v200 row_ror:1 row_mask:0xf bank_mask:0xf\n"
   "v_add_f32_dpp v210, v201, v201 row_ror:1 row_mask:0xf bank_mask:0xf\n"
   "v_mov_b32 v209, s48\n"
   "v_mov_b32_e64 v211, s48\n"
   "v_mul_f32 v1, 0xbb000000, v208\n"
   "v_mul_f32 v210, 0x3b000000, v210\n"
   "v_fma_f32 v210, -v1, v1, v210\n"
   "v_max_f32_e64 v210, 0, v210\n"
   "v_add_f32 v210, 0x3727c5ac, v210\n"
   "v_rsq_f32 v208, v210\n"
   "s_nop 0\n"
   "v_mul_f32 v210, v1, v208\n"
   "v_lshlrev_b32 v2, 6, v7\n"
   "v_lshl_or_b32 v2, v8, 4, v2\n"
   "s_lshl_b32 s56, s51, 7\n"
   "s_lshl1_add_u32 s56, s72, s56\n"
   "s_add_u32 s56, s56, 0xbff00\n"
   "s_mov_b32 exec_lo, 0x30003\n"
   "s_mov_b32 exec_hi, 0x30003\n"
   "buffer_store_dwordx4 v[208:211], v2, s[44:47], s56 offen sc1\n"
   "s_mov_b64 exec, -1\n"
   ".Lfb_skip5:\n"
   "s_cmp_eq_u32 s51, 1\n"
   "s_cbranch_scc0 .Lfb_skip6\n"
   "s_waitcnt vmcnt(6)\n"
   "v_readfirstlane_b32 s48, v10\n"
   "v_readfirstlane_b32 s49, v11\n"
   "s_add_u32 s48, s48, 0x7f4a7d95\n"
   "s_addc_u32 s49, s49, 0x9e3779b9\n"
   "s_and_b32 s86, s48, 0x3ff\n"
   "v_lshlrev_b32_e64 v1, 7, v6\n"
   "s_lshl_b32 s56, s53, 10\n"
   "s_add_u32 s56, s56, 0x1000\n"
   "s_nop 15\n"
   "s_mov_b64 exec, 0xff\n"
   "buffer_load_dword v212, v1, s[44:47], s56 offen sc1\n"
   "s_mov_b64 exec, -1\n"
   "buffer_load_dwordx4 v[76:79], v13, s[8:11], s59 offen offset:1024\n"
   "buffer_load_dwordx4 v[84:87], v13, s[8:11], s59 offen offset:2048\n"
   "buffer_load_dwordx4 v[92:95], v13, s[8:11], s59 offen offset:3072\n"
   "s_add_u32 s59, s70, 0x8000\n"
   "buffer_load_dwordx4 v[100:103], v13, s[8:11], s59 offen\n"
   "buffer_load_dwordx4 v[108:111], v13, s[8:11], s59 offen offset:1024\n"
   "buffer_load_dwordx4 v[116:119], v13, s[8:11], s59 offen offset:2048\n"
   "buffer_load_dwordx4 v[124:127], v13, s[8:11], s59 offen offset:3072\n"
   "s_add_u32 s59, s70, 0xc000\n"
   "buffer_load_dwordx4 v[132:135], v13, s[8:11], s59 offen\n"
   "buffer_load_dwordx4 v[140:143], v13, s[8:11], s59 offen offset:1024\n"
   ".Lfb_skip6:\n"
   "s_waitcnt vmcnt(13)\n"
   "v_lshlrev_b32 v32, 16, v36\n"
   "v_and_b32 v33, s65, v36\n"
   "v_lshlrev_b32 v34, 16, v37\n"
   "v_and_b32 v35, s65, v37\n"
   "v_lshlrev_b32 v36, 16, v38\n"
   "v_and_b32 v37, s65, v38\n"
   "v_lshlrev_b32 v38, 16, v39\n"
   "v_and_b32 v39, s65, v39\n"
   "s_waitcnt vmcnt(12)\n"
   "v_lshlrev_b32 v40, 16, v44\n"
   "v_and_b32 v41, s65, v44\n"
   "v_lshlrev_b32 v42, 16, v45\n"
   "v_and_b32 v43, s65, v45\n"
   "v_lshlrev_b32 v44, 16, v46\n"
   "v_and_b32 v45, s65, v46\n"
   "v_lshlrev_b32 v46, 16, v47\n"
   "v_and_b32 v47, s65, v47\n"
   "buffer_load_dwordx4 v[148:151], v13, s[8:11], s59 offen offset:2048\n"
   "buffer_load_dwordx4 v[156:159], v13, s[8:11], s59 offen offset:3072\n"
   "s_waitcnt vmcnt(13)\n"
   "v_lshlrev_b32 v48, 16, v52\n"
   "v_and_b32 v49, s65, v52\n"
   "v_lshlrev_b32 v50, 16, v53\n"
   "v_and_b32 v51, s65, v53\n"
   "v_lshlrev_b32 v52, 16, v54\n"
   "v_and_b32 v53, s65, v54\n"
   "v_lshlrev_b32 v54, 16, v55\n"
   "v_and_b32 v55, s65, v55\n"
   "s_waitcnt vmcnt(12)\n"
   "v_lshlrev_b32 v56, 16, v60\n"
   "v_and_b32 v57, s65, v60\n"
   "v_lshlrev_b32 v58, 16, v61\n"
   "v_and_b32 v59, s65, v61\n"
   "v_lshlrev_b32 v60, 16, v62\n"
   "v_and_b32 v61, s65, v62\n"
   "v_lshlrev_b32 v62, 16, v63\n"
   "v_and_b32 v63, s65, v63\n"
   "s_waitcnt vmcnt(11)\n"
   "v_lshlrev_b32 v64, 16, v68\n"
   "v_and_b32 v65, s65, v68\n"
   "v_lshlrev_b32 v66, 16, v69\n"
   "v_and_b32 v67, s65, v69\n"
   "v_lshlrev_b32 v68, 16, v70\n"
   "v_and_b32 v69, s65, v70\n"
   "v_lshlrev_b32 v70, 16, v71\n"
   "v_and_b32 v71, s65, v71\n"
   "s_waitcnt vmcnt(10)\n"
   "v_lshlrev_b32 v72, 16, v76\n"
   "v_and_b32 v73, s65, v76\n"
   "v_lshlrev_b32 v74, 16, v77\n"
   "v_and_b32 v75, s65, v77\n"
   "v_lshlrev_b32 v76, 16, v78\n"
   "v_and_b32 v77, s65, v78\n"
   "v_lshlrev_b32 v78, 16, v79\n"
   "v_and_b32 v79, s65, v79\n"
   "s_waitcnt vmcnt(9)\n"
   "v_lshlrev_b32 v80, 16, v84\n"
   "v_and_b32 v81, s65, v84\n"
   "v_lshlrev_b32 v82, 16, v85\n"
   "v_and_b32 v83, s65, v85\n"
   "v_lshlrev_b32 v84, 16, v86\n"
   "v_and_b32 v85, s65, v86\n"
   "v_lshlrev_b32 v86, 16, v87\n"
   "v_and_b32 v87, s65, v87\n"
   "s_waitcnt vmcnt(8)\n"
   "v_lshlrev_b32 v88, 16, v92\n"
   "v_and_b32 v89, s65, v92\n"
   "v_lshlrev_b32 v90, 16, v93\n"
   "v_and_b32 v91, s65, v93\n"
   "v_lshlrev_b32 v92, 16, v94\n"
   "v_and_b32 v93, s65, v94\n"
   "v_lshlrev_b32 v94, 16, v95\n"
   "v_and_b32 v95, s65, v95\n"
   "s_waitcnt vmcnt(7)\n"
   "v_lshlrev_b32 v96, 16, v100\n"
   "v_and_b32 v97, s65, v100\n"
   "v_lshlrev_b32 v98, 16, v101\n"
   "v_and_b32 v99, s65, v101\n"
   "v_lshlrev_b32 v100, 16, v102\n"
   "v_and_b32 v101, s65, v102\n"
   "v_lshlrev_b32 v102, 16, v103\n"
   "v_and_b32_e64 v103, s65, v103\n"
   "buffer_load_dword v5, v184, s[12:15], s82 offen\n"
   "s_waitcnt vmcnt(7)\n"
   "v_lshlrev_b32 v104, 16, v108\n"
   "v_and_b32 v105, s65, v108\n"
   "v_lshlrev_b32 v106, 16, v109\n"
   "v_and_b32 v107, s65, v109\n"
   "v_lshlrev_b32 v108, 16, v110\n"
   "v_and_b32 v109, s65, v110\n"
   "v_lshlrev_b32 v110, 16, v111\n"
   "v_and_b32 v111, s65, v111\n"
   "s_waitcnt vmcnt(6)\n"
   "v_lshlrev_b32 v112, 16, v116\n"
   "v_and_b32 v113, s65, v116\n"
   "v_lshlrev_b32 v114, 16, v117\n"
   "v_and_b32 v115, s65, v117\n"
   "v_lshlrev_b32 v116, 16, v118\n"
   "v_and_b32 v117, s65, v118\n"
   "v_lshlrev_b32 v118, 16, v119\n"
   "v_and_b32 v119, s65, v119\n"
   "s_waitcnt vmcnt(5)\n"
   "v_lshlrev_b32 v120, 16, v124\n"
   "v_and_b32 v121, s65, v124\n"
   "v_lshlrev_b32 v122, 16, v125\n"
   "v_and_b32 v123, s65, v125\n"
   "v_lshlrev_b32 v124, 16, v126\n"
   "v_and_b32 v125, s65, v126\n"
   "v_lshlrev_b32 v126, 16, v127\n"
   "v_and_b32 v127, s65, v127\n"
   "s_add_u32 m0, s80, 0x10000\n"
   "s_add_u32 s58, s75, 0\n"
   "buffer_load_dwordx4 v161, s[12:15], s58 offen lds\n"
   "buffer_load_dwordx4 v161, s[12:15], s58 offen offset:1024 lds\n"
   "buffer_load_dwordx4 v161, s[12:15], s58 offen offset:2048 lds\n"
   "s_add_u32 m0, s80, 0x13000\n"
   "s_add_u32 s58, s75, 0x18000\n"
   "buffer_load_dwordx4 v161, s[12:15], s58 offen lds\n"
   "buffer_load_dwordx4 v161, s[12:15], s58 offen offset:1024 lds\n"
   "buffer_load_dwordx4 v161, s[12:15], s58 offen offset:2048 lds\n"
   "s_waitcnt vmcnt(10)\n"
   "v_lshlrev_b32 v128, 16, v132\n"
   "v_and_b32 v129, s65, v132\n"
   "v_lshlrev_b32 v130, 16, v133\n"
   "v_and_b32 v131, s65, v133\n"
   "v_lshlrev_b32 v132, 16, v134\n"
   "v_and_b32 v133, s65, v134\n"
   "v_lshlrev_b32 v134, 16, v135\n"
   "v_and_b32 v135, s65, v135\n"
   "s_waitcnt vmcnt(9)\n"
   "v_lshlrev_b32 v136, 16, v140\n"
   "v_and_b32 v137, s65, v140\n"
   "v_lshlrev_b32 v138, 16, v141\n"
   "v_and_b32 v139, s65, v141\n"
   "v_lshlrev_b32 v140, 16, v142\n"
   "v_and_b32 v141, s65, v142\n"
   "v_lshlrev_b32 v142, 16, v143\n"
   "v_and_b32 v143, s65, v143\n"
   "s_waitcnt vmcnt(8)\n"
   "v_lshlrev_b32 v144, 16, v148\n"
   "v_and_b32 v145, s65, v148\n"
   "v_lshlrev_b32 v146, 16, v149\n"
   "v_and_b32 v147, s65, v149\n"
   "v_lshlrev_b32 v148, 16, v150\n"
   "v_and_b32 v149, s65, v150\n"
   "v_lshlrev_b32 v150, 16, v151\n"
   "v_and_b32 v151, s65, v151\n"
   "s_waitcnt vmcnt(7)\n"
   "v_lshlrev_b32 v152, 16, v156\n"
   "v_and_b32 v153, s65, v156\n"
   "v_lshlrev_b32 v154, 16, v157\n"
   "v_and_b32 v155, s65, v157\n"
   "v_lshlrev_b32 v156, 16, v158\n"
   "v_and_b32 v157, s65, v158\n"
   "v_lshlrev_b32 v158, 16, v159\n"
   "v_and_b32 v159, s65, v159\n"
   "v_lshl_add_u32 v3, v8, 2, v163\n"
   "v_lshl_add_u32 v3, v3, 5, s61\n"
   "ds_read_b64_tr_b16 v[200:201], v3 offset:1024\n"
   "ds_read_b64_tr_b16 v[202:203], v3 offset:1032\n"
   "ds_read_b64_tr_b16 v[204:205], v3 offset:1040\n"
   "ds_read_b64_tr_b16 v[206:207], v3 offset:1048\n"
   "v_lshlrev_b32_e64 v12, 16, v12\n"
   "v_mul_f32 v1, 0xbfb8aa3b, v12\n"
   "v_exp_f32 v1, v1\n"
   "s_nop 0\n"
   "v_add_f32 v1, 1.0, v1\n"
   "v_rcp_f32 v1, v1\n"
   "s_nop 0\n"
   "v_mul_f32 v1, v12, v1\n"
   "v_cvt_pk_bf16_f32 v12, v1, 0\n"
   "v_lshl_add_u32 v1, v6, 1, s61\n"
   "ds_write_b16 v1, v12 offset:1536\n"
   "v_and_b32_e64 v1, 1, v8\n"
   "v_lshl_or_b32 v1, v182, 2, v1\n"
   "v_lshl_add_u32 v1, v1, 3, s61\n"
   "ds_read_b64 a[212:213], v1 offset:1536\n"
   "ds_read_b64 a[214:215], v1 offset:1552\n"
   "ds_read_b64 a[216:217], v1 offset:1600\n"
   "ds_read_b64 a[218:219], v1 offset:1616\n"
   "v_bfe_i32 v2, v7, 2, 1\n"
   "v_and_b32 v2, 0x80008000, v2\n"
   "v_bfe_i32 v3, v7, 3, 1\n"
   "v_not_b32 v4, v3\n"
   "s_waitcnt lgkmcnt(8)\n"
   "v_bitop3_b32 v1, v200, v2, v3 bitop3:0x28\n"
   "v_bitop3_b32 v12, v200, v2, v4 bitop3:0x28\n"
   "v_accvgpr_write_b32 a194, v1\n"
   "v_accvgpr_write_b32 a192, v12\n"
   "v_bitop3_b32 v1, v201, v2, v3 bitop3:0x28\n"
   "v_bitop3_b32 v12, v201, v2, v4 bitop3:0x28\n"
   "v_accvgpr_write_b32 a195, v1\n"
   "v_accvgpr_write_b32 a193, v12\n"
   "s_waitcnt lgkmcnt(7)\n"
   "s_nop 0\n"
   "v_bitop3_b32 v1, v202, v2, v3 bitop3:0x28\n"
   "v_bitop3_b32 v12, v202, v2, v4 bitop3:0x28\n"
   "v_accvgpr_write_b32 a198, v1\n"
   "v_accvgpr_write_b32 a196, v12\n"
   "v_bitop3_b32 v1, v203, v2, v3 bitop3:0x28\n"
   "v_bitop3_b32 v12, v203, v2, v4 bitop3:0x28\n"
   "v_accvgpr_write_b32 a199, v1\n"
   "v_accvgpr_write_b32 a197, v12\n"
   "s_waitcnt lgkmcnt(6)\n"
   "v_bitop3_b32 v1, v204, v2, v3 bitop3:0x28\n"
   "v_bitop3_b32 v12, v204, v2, v4 bitop3:0x28\n"
   "v_accvgpr_write_b32 a202, v1\n"
   "v_accvgpr_write_b32 a200, v12\n"
   "v_bitop3_b32 v1, v205, v2, v3 bitop3:0x28\n"
   "v_bitop3_b32 v12, v205, v2, v4 bitop3:0x28\n"
   "v_accvgpr_write_b32 a203, v1\n"
   "v_accvgpr_write_b32 a201, v12\n"
   "s_waitcnt lgkmcnt(5)\n"
   "v_bitop3_b32 v1, v206, v2, v3 bitop3:0x28\n"
   "v_bitop3_b32 v12, v206, v2, v4 bitop3:0x28\n"
   "v_accvgpr_write_b32 a206, v1\n"
   "v_accvgpr_write_b32 a204, v12\n"
   "v_bitop3_b32 v1, v207, v2, v3 bitop3:0x28\n"
   "v_bitop3_b32 v12, v207, v2, v4 bitop3:0x28\n"
   "v_accvgpr_write_b32 a207, v1\n"
   "v_accvgpr_write_b32 a205, v12\n"
   "s_cmp_eq_u32 s51, 1\n"
   "s_cbranch_scc0 .Lfb_skip8\n"
   "v_subrev_u32 v212, s68, v212\n"
   "v_add_u32 v1, 0x12c, v212\n"
   "v_cmp_gt_u32 vcc, 0x258, v1\n"
   "v_mov_b32 v2, 40\n"
   "s_mov_b32 s57, 0\n"
   "v_cndmask_b32 v212, v2, v212, vcc\n"
   "s_nop 0\n"
   "v_readlane_b32 s56, v212, 0\n"
   "s_nop 0\n"
   "s_max_i32 s57, s57, s56\n"
   "v_readlane_b32 s56, v212, 1\n"
   "s_nop 0\n"
   "s_max_i32 s57, s57, s56\n"
   "v_readlane_b32 s56, v212, 2\n"
   "s_nop 0\n"
   "s_max_i32 s57, s57, s56\n"
   "v_readlane_b32 s56, v212, 3\n"
   "s_nop 0\n"
   "s_max_i32 s57, s57, s56\n"
   "v_readlane_b32 s56, v212, 4\n"
   "s_nop 0\n"
   "s_max_i32 s57, s57, s56\n"
   "v_readlane_b32 s56, v212, 5\n"
   "s_nop 0\n"
   "s_max_i32 s57, s57, s56\n"
   "v_readlane_b32 s56, v212, 6\n"
   "s_nop 0\n"
   "s_max_i32 s57, s57, s56\n"
   "v_readlane_b32 s56, v212, 7\n"
   "s_nop 0\n"
   "s_max_i32 s57, s57, s56\n"
   "s_min_i32 s57, s57, 0x64\n"
   "s_add_u32 s57, s57, 0xb4\n"
   "s_mov_b32 s56, s68\n"
   ".Lfb_pace7:\n"
   "s_memrealtime s[68:69]\n"
   "s_waitcnt lgkmcnt(0)\n"
   "s_sub_u32 s60, s68, s56\n"
   "s_cmp_lt_u32 s60, s57\n"
   "s_cbranch_scc1 .Lfb_pace7\n"
   ".Lfb_skip8:\n"
   "s_barrier\n"
   "s_lshr_b32 s57, s70, 5\n"
   "s_add_u32 s57, s57, 0xc0000\n"
   "buffer_load_dwordx4 v[224:227], v179, s[44:47], s57 offen sc1\n"
   "s_add_u32 s57, s81, 0x20000\n"
   "buffer_load_dwordx4 v[16:19], v178, s[44:47], s57 offen sc1\n"
   "s_add_u32 s57, s81, 0x23000\n"
   "buffer_load_dwordx4 v[20:23], v178, s[44:47], s57 offen sc1\n"
   "s_add_u32 s57, s81, 0x26000\n"
   "buffer_load_dwordx4 v[24:27], v178, s[44:47], s57 offen sc1\n"
   "s_add_u32 s57, s81, 0x29000\n"
   "buffer_load_dwordx4 v[28:31], v178, s[44:47], s57 offen sc1\n"
   "s_add_u32 s57, s81, 0x2c000\n"
   "buffer_load_dwordx4 v[192:195], v178, s[44:47], s57 offen sc1\n"
   "s_add_u32 s57, s81, 0x2f000\n"
   "buffer_load_dwordx4 v[196:199], v178, s[44:47], s57 offen sc1\n"
   "s_add_u32 s57, s81, 0x32000\n"
   "buffer_load_dwordx4 v[216:219], v178, s[44:47], s57 offen sc1\n"
   "s_add_u32 s57, s81, 0x35000\n"
   "buffer_load_dwordx4 v[220:223], v178, s[44:47], s57 offen sc1\n"
   "s_waitcnt vmcnt(8)\n"
   "v_xor_b32 v10, s48, v225\n"
   "v_bitop3_b32 v10, v227, s48, v10 bitop3:0xbe\n"
   "v_cmp_ne_u32 vcc, 0, v10\n"
   "s_nop 0\n"
   "s_cbranch_vccnz .Lfb_stub9\n"
   ".Lfb_back10:\n"
   "v_mov_b32_e64 v225, v226\n"
   "ds_write_b64 v174, v[224:225] offset:3072\n"
   "ds_read_b128 v[228:231], v176 offset:3072\n"
   "ds_read_b128 v[232:235], v176 offset:3088\n"
   "ds_read_b128 v[236:239], v176 offset:3200\n"
   "ds_read_b128 v[240:243], v176 offset:3216\n"
   "s_add_u32 m0, s80, 0x16000\n"
   "s_add_u32 s58, s75, 0x30000\n"
   "buffer_load_dwordx4 v161, s[12:15], s58 offen lds\n"
   "buffer_load_dwordx4 v161, s[12:15], s58 offen offset:1024 lds\n"
   "buffer_load_dwordx4 v161, s[12:15], s58 offen offset:2048 lds\n"
   "s_add_u32 m0, s80, 0x19000\n"
   "s_add_u32 s58, s75, 0x48000\n"
   "buffer_load_dwordx4 v161, s[12:15], s58 offen lds\n"
   "buffer_load_dwordx4 v161, s[12:15], s58 offen offset:1024 lds\n"
   "buffer_load_dwordx4 v161, s[12:15], s58 offen offset:2048 lds\n"
   "s_waitcnt lgkmcnt(3)\n"
   "v_pk_fma_f32 v[32:33], v[32:33], v[228:229], v[228:229] op_sel:[0,0,1] op_sel_hi:[1,0,1]\n"
   "v_pk_fma_f32 v[34:35], v[34:35], v[228:229], v[228:229] op_sel:[0,0,1] op_sel_hi:[1,0,1]\n"
   "v_pk_fma_f32 v[36:37], v[36:37], v[228:229], v[228:229] op_sel:[0,0,1] op_sel_hi:[1,0,1]\n"
   "v_pk_fma_f32 v[38:39], v[38:39], v[228:229], v[228:229] op_sel:[0,0,1] op_sel_hi:[1,0,1]\n"
   "v_pk_fma_f32 v[40:41], v[40:41], v[230:231], v[230:231] op_sel:[0,0,1] op_sel_hi:[1,0,1]\n"
   "v_pk_fma_f32 v[42:43], v[42:43], v[230:231], v[230:231] op_sel:[0,0,1] op_sel_hi:[1,0,1]\n"
   "v_pk_fma_f32 v[44:45], v[44:45], v[230:231], v[230:231] op_sel:[0,0,1] op_sel_hi:[1,0,1]\n"
   "v_pk_fma_f32 v[46:47], v[46:47], v[230:231], v[230:231] op_sel:[0,0,1] op_sel_hi:[1,0,1]\n"
   "s_waitcnt lgkmcnt(2)\n"
   "v_pk_fma_f32 v[48:49], v[48:49], v[232:233], v[232:233] op_sel:[0,0,1] op_sel_hi:[1,0,1]\n"
   "v_pk_fma_f32 v[50:51], v[50:51], v[232:233], v[232:233] op_sel:[0,0,1] op_sel_hi:[1,0,1]\n"
   "v_pk_fma_f32 v[52:53], v[52:53], v[232:233], v[232:233] op_sel:[0,0,1] op_sel_hi:[1,0,1]\n"
   "v_pk_fma_f32 v[54:55], v[54:55], v[232:233], v[232:233] op_sel:[0,0,1] op_sel_hi:[1,0,1]\n"
   "v_pk_fma_f32 v[56:57], v[56:57], v[234:235], v[234:235] op_sel:[0,0,1] op_sel_hi:[1,0,1]\n"
   "v_pk_fma_f32 v[58:59], v[58:59], v[234:235], v[234:235] op_sel:[0,0,1] op_sel_hi:[1,0,1]\n"
   "v_pk_fma_f32 v[60:61], v[60:61], v[234:235], v[234:235] op_sel:[0,0,1] op_sel_hi:[1,0,1]\n"
   "v_pk_fma_f32 v[62:63], v[62:63], v[234:235], v[234:235] op_sel:[0,0,1] op_sel_hi:[1,0,1]\n"
   "s_waitcnt lgkmcnt(1)\n"
   "v_pk_fma_f32 v[64:65], v[64:65], v[236:237], v[236:237] op_sel:[0,0,1] op_sel_hi:[1,0,1]\n"
   "v_pk_fma_f32 v[66:67], v[66:67], v[236:237], v[236:237] op_sel:[0,0,1] op_sel_hi:[1,0,1]\n"
   "v_pk_fma_f32 v[68:69], v[68:69], v[236:237], v[236:237] op_sel:[0,0,1] op_sel_hi:[1,0,1]\n"
   "v_pk_fma_f32 v[70:71], v[70:71], v[236:237], v[236:237] op_sel:[0,0,1] op_sel_hi:[1,0,1]\n"
   "v_pk_fma_f32 v[72:73], v[72:73], v[238:239], v[238:239] op_sel:[0,0,1] op_sel_hi:[1,0,1]\n"
   "v_pk_fma_f32 v[74:75], v[74:75], v[238:239], v[238:239] op_sel:[0,0,1] op_sel_hi:[1,0,1]\n"
   "v_pk_fma_f32 v[76:77], v[76:77], v[238:239], v[238:239] op_sel:[0,0,1] op_sel_hi:[1,0,1]\n"
   "v_pk_fma_f32 v[78:79], v[78:79], v[238:239], v[238:239] op_sel:[0,0,1] op_sel_hi:[1,0,1]\n"
   "s_waitcnt lgkmcnt(0)\n"
   "v_pk_fma_f32 v[80:81], v[80:81], v[240:241], v[240:241] op_sel:[0,0,1] op_sel_hi:[1,0,1]\n"
   "v_pk_fma_f32 v[82:83], v[82:83], v[240:241], v[240:241] op_sel:[0,0,1] op_sel_hi:[1,0,1]\n"
   "v_pk_fma_f32 v[84:85], v[84:85], v[240:241], v[240:241] op_sel:[0,0,1] op_sel_hi:[1,0,1]\n"
   "v_pk_fma_f32 v[86:87], v[86:87], v[240:241], v[240:241] op_sel:[0,0,1] op_sel_hi:[1,0,1]\n"
   "v_pk_fma_f32 v[88:89], v[88:89], v[242:243], v[242:243] op_sel:[0,0,1] op_sel_hi:[1,0,1]\n"
   "v_pk_fma_f32 v[90:91], v[90:91], v[242:243], v[242:243] op_sel:[0,0,1] op_sel_hi:[1,0,1]\n"
   "v_pk_fma_f32 v[92:93], v[92:93], v[242:243], v[242:243] op_sel:[0,0,1] op_sel_hi:[1,0,1]\n"
   "v_pk_fma_f32 v[94:95], v[94:95], v[242:243], v[242:243] op_sel:[0,0,1] op_sel_hi:[1,0,1]\n"
   "ds_read_b128 v[228:231], v176 offset:3328\n"
   "ds_read_b128 v[232:235], v176 offset:3344\n"
   "ds_read_b128 v[236:239], v176 offset:3456\n"
   "ds_read_b128 v[240:243], v176 offset:3472\n"
   "s_waitcnt lgkmcnt(3)\n"
   "v_pk_fma_f32 v[96:97], v[96:97], v[228:229], v[228:229] op_sel:[0,0,1] op_sel_hi:[1,0,1]\n"
   "v_pk_fma_f32 v[98:99], v[98:99], v[228:229], v[228:229] op_sel:[0,0,1] op_sel_hi:[1,0,1]\n"
   "v_pk_fma_f32 v[100:101], v[100:101], v[228:229], v[228:229] op_sel:[0,0,1] op_sel_hi:[1,0,1]\n"
   "v_pk_fma_f32 v[102:103], v[102:103], v[228:229], v[228:229] op_sel:[0,0,1] op_sel_hi:[1,0,1]\n"
   "v_pk_fma_f32 v[104:105], v[104:105], v[230:231], v[230:231] op_sel:[0,0,1] op_sel_hi:[1,0,1]\n"
   "v_pk_fma_f32 v[106:107], v[106:107], v[230:231], v[230:231] op_sel:[0,0,1] op_sel_hi:[1,0,1]\n"
   "v_pk_fma_f32 v[108:109], v[108:109], v[230:231], v[230:231] op_sel:[0,0,1] op_sel_hi:[1,0,1]\n"
   "v_pk_fma_f32 v[110:111], v[110:111], v[230:231], v[230:231] op_sel:[0,0,1] op_sel_hi:[1,0,1]\n"
   "s_waitcnt lgkmcnt(2)\n"
   "v_pk_fma_f32 v[112:113], v[112:113], v[232:233], v[232:233] op_sel:[0,0,1] op_sel_hi:[1,0,1]\n"
   "v_pk_fma_f32 v[114:115], v[114:115], v[232:233], v[232:233] op_sel:[0,0,1] op_sel_hi:[1,0,1]\n"
   "v_pk_fma_f32 v[116:117], v[116:117], v[232:233], v[232:233] op_sel:[0,0,1] op_sel_hi:[1,0,1]\n"
   "v_pk_fma_f32 v[118:119], v[118:119], v[232:233], v[232:233] op_sel:[0,0,1] op_sel_hi:[1,0,1]\n"
   "v_pk_fma_f32 v[120:121], v[120:121], v[234:235], v[234:235] op_sel:[0,0,1] op_sel_hi:[1,0,1]\n"
   "v_pk_fma_f32 v[122:123], v[122:123], v[234:235], v[234:235] op_sel:[0,0,1] op_sel_hi:[1,0,1]\n"
   "v_pk_fma_f32 v[124:125], v[124:125], v[234:235], v[234:235] op_sel:[0,0,1] op_sel_hi:[1,0,1]\n"
   "v_pk_fma_f32 v[126:127], v[126:127], v[234:235], v[234:235] op_sel:[0,0,1] op_sel_hi:[1,0,1]\n"
   "s_waitcnt lgkmcnt(1)\n"
   "v_pk_fma_f32 v[128:129], v[128:129], v[236:237], v[236:237] op_sel:[0,0,1] op_sel_hi:[1,0,1]\n"
   "v_pk_fma_f32 v[130:131], v[130:131], v[236:237], v[236:237] op_sel:[0,0,1] op_sel_hi:[1,0,1]\n"
   "v_pk_fma_f32 v[132:133], v[132:133], v[236:237], v[236:237] op_sel:[0,0,1] op_sel_hi:[1,0,1]\n"
   "v_pk_fma_f32 v[134:135], v[134:135], v[236:237], v[236:237] op_sel:[0,0,1] op_sel_hi:[1,0,1]\n"
   "v_pk_fma_f32 v[136:137], v[136:137], v[238:239], v[238:239] op_sel:[0,0,1] op_sel_hi:[1,0,1]\n"
   "v_pk_fma_f32 v[138:139], v[138:139], v[238:239], v[238:239] op_sel:[0,0,1] op_sel_hi:[1,0,1]\n"
   "v_pk_fma_f32 v[140:141], v[140:141], v[238:239], v[238:239] op_sel:[0,0,1] op_sel_hi:[1,0,1]\n"
   "v_pk_fma_f32 v[142:143], v[142:143], v[238:239], v[238:239] op_sel:[0,0,1] op_sel_hi:[1,0,1]\n"
   "s_waitcnt lgkmcnt(0)\n"
   "v_pk_fma_f32 v[144:145], v[144:145], v[240:241], v[240:241] op_sel:[0,0,1] op_sel_hi:[1,0,1]\n"
   "v_pk_fma_f32 v[146:147], v[146:147], v[240:241], v[240:241] op_sel:[0,0,1] op_sel_hi:[1,0,1]\n"
   "v_pk_fma_f32 v[148:149], v[148:149], v[240:241], v[240:241] op_sel:[0,0,1] op_sel_hi:[1,0,1]\n"
   "v_pk_fma_f32 v[150:151], v[150:151], v[240:241], v[240:241] op_sel:[0,0,1] op_sel_hi:[1,0,1]\n"
   "v_pk_fma_f32 v[152:153], v[152:153], v[242:243], v[242:243] op_sel:[0,0,1] op_sel_hi:[1,0,1]\n"
   "v_pk_fma_f32 v[154:155], v[154:155], v[242:243], v[242:243] op_sel:[0,0,1] op_sel_hi:[1,0,1]\n"
   "v_pk_fma_f32 v[156:157], v[156:157], v[242:243], v[242:243] op_sel:[0,0,1] op_sel_hi:[1,0,1]\n"
   "v_pk_fma_f32 v[158:159], v[158:159], v[242:243], v[242:243] op_sel:[0,0,1] op_sel_hi:[1,0,1]\n"
   "s_waitcnt vmcnt(13)\n"
   "v_xor_b32 v16, s86, v16\n"
   "v_xor_b32 v18, s86, v18\n"
   "s_waitcnt vmcnt(12)\n"
   "v_xor_b32 v20, s86, v20\n"
   "v_xor_b32 v22, s86, v22\n"
   "v_pk_add_f32 v[200:201], v[16:17], v[20:21]\n"
   "v_pk_add_f32 v[202:203], v[18:19], v[22:23]\n"
   "s_waitcnt vmcnt(11)\n"
   "v_xor_b32 v24, s86, v24\n"
   "v_xor_b32_e64 v26, s86, v26\n"
   "v_pk_add_f32 v[200:201], v[200:201], v[24:25]\n"
   "v_pk_add_f32 v[202:203], v[202:203], v[26:27]\n"
   "s_waitcnt vmcnt(10)\n"
   "v_xor_b32 v28, s86, v28\n"
   "v_xor_b32_e64 v30, s86, v30\n"
   "v_pk_add_f32 v[200:201], v[200:201], v[28:29]\n"
   "v_pk_add_f32 v[202:203], v[202:203], v[30:31]\n"
   "s_waitcnt vmcnt(9)\n"
   "v_xor_b32 v192, s86, v192\n"
   "v_xor_b32_e64 v194, s86, v194\n"
   "v_pk_add_f32 v[200:201], v[200:201], v[192:193]\n"
   "v_pk_add_f32 v[202:203], v[202:203], v[194:195]\n"
   "s_waitcnt vmcnt(8)\n"
   "v_xor_b32 v196, s86, v196\n"
   "v_xor_b32_e64 v198, s86, v198\n"
   "v_pk_add_f32 v[200:201], v[200:201], v[196:197]\n"
   "v_pk_add_f32 v[202:203], v[202:203], v[198:199]\n"
   "s_waitcnt vmcnt(7)\n"
   "v_xor_b32 v216, s86, v216\n"
   "v_xor_b32_e64 v218, s86, v218\n"
   "v_pk_add_f32 v[200:201], v[200:201], v[216:217]\n"
   "v_pk_add_f32 v[202:203], v[202:203], v[218:219]\n"
   "s_waitcnt vmcnt(6)\n"
   "v_xor_b32 v220, s86, v220\n"
   "v_xor_b32_e64 v222, s86, v222\n"
   "v_pk_add_f32 v[200:201], v[200:201], v[220:221]\n"
   "v_pk_add_f32 v[202:203], v[202:203], v[222:223]\n"
   "v_cvt_pk_bf16_f32 v204, v200, v201\n"
   "v_cvt_pk_bf16_f32 v205, v202, v203\n"
   "v_lshlrev_b32 v200, 16, v204\n"
   "v_and_b32 v201, s65, v204\n"
   "v_lshlrev_b32 v202, 16, v205\n"
   "v_and_b32 v203, s65, v205\n"
   "v_pk_add_f32 v[200:201], v[200:201], v[180:181] op_sel_hi:[1,0]\n"
   "v_pk_add_f32 v[202:203], v[202:203], v[180:181] op_sel_hi:[1,0]\n"
   "v_cvt_pk_bf16_f32 v204, v200, v201\n"
   "v_cvt_pk_bf16_f32 v205, v202, v203\n"
   "v_lshlrev_b32 v200, 16, v204\n"
   "v_and_b32 v201, s65, v204\n"
   "v_lshlrev_b32 v202, 16, v205\n"
   "v_and_b32 v203, s65, v205\n"
   "ds_write_b128 v177, v[200:203] offset:2048\n"
   "ds_read_b128 v[228:231], v175 offset:2560\n"
   "ds_read_b128 v[232:235], v175 offset:2576\n"
   "ds_read_b128 v[236:239], v175 offset:2048\n"
   "ds_read_b128 v[240:243], v175 offset:2064\n"
   "s_add_u32 m0, s80, 0x1c000\n"
   "s_add_u32 s58, s76, 0\n"
   "s_nop 0\n"
   "buffer_load_dwordx4 v161, s[12:15], s58 offen lds\n"
   "buffer_load_dwordx4 v161, s[12:15], s58 offen offset:1024 lds\n"
   "buffer_load_dwordx4 v161, s[12:15], s58 offen offset:2048 lds\n"
   "s_add_u32 m0, s80, 0x1f000\n"
   "s_add_u32 s58, s76, 0x18000\n"
   "buffer_load_dwordx4 v161, s[12:15], s58 offen lds\n"
   "buffer_load_dwordx4 v161, s[12:15], s58 offen offset:1024 lds\n"
   "buffer_load_dwordx4 v161, s[12:15], s58 offen offset:2048 lds\n"
   "v_or3_b32 v10, v16, v18, v20\n"
   "v_or3_b32 v10, v10, v22, v24\n"
   "v_or3_b32 v10, v10, v26, v28\n"
   "v_or3_b32 v10, v10, v30, v192\n"
   "v_or3_b32 v10, v10, v194, v196\n"
   "v_or3_b32 v10, v10, v198, v216\n"
   "v_or3_b32 v10, v10, v218, v220\n"
   "v_or3_b32 v10, v10, v222, v222\n"
   "v_and_b32_e64 v10, v15, v10\n"
   "v_cmp_ne_u32 vcc, 0, v10\n"
   "s_nop 0\n"
   "s_cbranch_vccnz .Lfb_stub11\n"
   ".Lfb_back12:\n"
   "s_waitcnt lgkmcnt(1)\n"
   "v_pk_fma_f32 v[32:33], v[32:33], v[228:229], v[236:237]\n"
   "v_pk_fma_f32 v[34:35], v[34:35], v[230:231], v[238:239]\n"
   "s_waitcnt lgkmcnt(0)\n"
   "v_pk_fma_f32 v[36:37], v[36:37], v[232:233], v[240:241]\n"
   "v_pk_fma_f32 v[38:39], v[38:39], v[234:235], v[242:243]\n"
   "v_cvt_pk_bf16_f32 v32, v32, v33\n"
   "v_cvt_pk_bf16_f32 v33, v34, v35\n"
   "v_cvt_pk_bf16_f32 v34, v36, v37\n"
   "v_cvt_pk_bf16_f32 v35, v38, v39\n"
   "ds_write_b128 v168, v[32:35]\n"
   "v_pk_fma_f32 v[40:41], v[40:41], v[228:229], v[236:237]\n"
   "v_pk_fma_f32 v[42:43], v[42:43], v[230:231], v[238:239]\n"
   "v_pk_fma_f32 v[44:45], v[44:45], v[232:233], v[240:241]\n"
   "v_pk_fma_f32 v[46:47], v[46:47], v[234:235], v[242:243]\n"
   "v_cvt_pk_bf16_f32 v40, v40, v41\n"
   "v_cvt_pk_bf16_f32 v41, v42, v43\n"
   "v_cvt_pk_bf16_f32 v42, v44, v45\n"
   "v_cvt_pk_bf16_f32 v43, v46, v47\n"
   "ds_write_b128 v169, v[40:43]\n"
   "v_pk_fma_f32 v[48:49], v[48:49], v[228:229], v[236:237]\n"
   "v_pk_fma_f32 v[50:51], v[50:51], v[230:231], v[238:239]\n"
   "v_pk_fma_f32 v[52:53], v[52:53], v[232:233], v[240:241]\n"
   "v_pk_fma_f32 v[54:55], v[54:55], v[234:235], v[242:243]\n"
   "v_cvt_pk_bf16_f32 v48, v48, v49\n"
   "v_cvt_pk_bf16_f32 v49, v50, v51\n"
   "v_cvt_pk_bf16_f32 v50, v52, v53\n"
   "v_cvt_pk_bf16_f32 v51, v54, v55\n"
   "ds_write_b128 v170, v[48:51]\n"
   "s_mul_i32 s59, s2, 0x1580\n"
   "buffer_load_dword v5, v185, s[24:27], s59 offen\n"
   "s_lshl_b32 s59, s2, 11\n"
   "s_nop 0\n"
   "buffer_load_dword v5, v186, s[20:23], s59 offen\n"
   "s_mul_i32 s59, s2, 0x1580\n"
   "buffer_load_dword v5, v185, s[28:31], s59 offen\n"
   "v_pk_fma_f32 v[56:57], v[56:57], v[228:229], v[236:237]\n"
   "v_pk_fma_f32 v[58:59], v[58:59], v[230:231], v[238:239]\n"
   "v_pk_fma_f32 v[60:61], v[60:61], v[232:233], v[240:241]\n"
   "v_pk_fma_f32 v[62:63], v[62:63], v[234:235], v[242:243]\n"
   "v_cvt_pk_bf16_f32 v56, v56, v57\n"
   "v_cvt_pk_bf16_f32 v57, v58, v59\n"
   "v_cvt_pk_bf16_f32 v58, v60, v61\n"
   "v_cvt_pk_bf16_f32 v59, v62, v63\n"
   "ds_write_b128 v171, v[56:59]\n"
   "v_pk_fma_f32 v[64:65], v[64:65], v[228:229], v[236:237]\n"
   "v_pk_fma_f32 v[66:67], v[66:67], v[230:231], v[238:239]\n"
   "v_pk_fma_f32 v[68:69], v[68:69], v[232:233], v[240:241]\n"
   "v_pk_fma_f32 v[70:71], v[70:71], v[234:235], v[242:243]\n"
   "v_cvt_pk_bf16_f32 v64, v64, v65\n"
   "v_cvt_pk_bf16_f32 v65, v66, v67\n"
   "v_cvt_pk_bf16_f32 v66, v68, v69\n"
   "v_cvt_pk_bf16_f32 v67, v70, v71\n"
   "ds_write_b128 v168, v[64:67] offset:16384\n"
   "v_pk_fma_f32 v[72:73], v[72:73], v[228:229], v[236:237]\n"
   "v_pk_fma_f32 v[74:75], v[74:75], v[230:231], v[238:239]\n"
   "v_pk_fma_f32 v[76:77], v[76:77], v[232:233], v[240:241]\n"
   "v_pk_fma_f32 v[78:79], v[78:79], v[234:235], v[242:243]\n"
   "v_cvt_pk_bf16_f32 v72, v72, v73\n"
   "v_cvt_pk_bf16_f32 v73, v74, v75\n"
   "v_cvt_pk_bf16_f32 v74, v76, v77\n"
   "v_cvt_pk_bf16_f32 v75, v78, v79\n"
   "ds_write_b128 v169, v[72:75] offset:16384\n"
   "v_pk_fma_f32 v[80:81], v[80:81], v[228:229], v[236:237]\n"
   "v_pk_fma_f32 v[82:83], v[82:83], v[230:231], v[238:239]\n"
   "v_pk_fma_f32 v[84:85], v[84:85], v[232:233], v[240:241]\n"
   "v_pk_fma_f32 v[86:87], v[86:87], v[234:235], v[242:243]\n"
   "v_cvt_pk_bf16_f32 v80, v80, v81\n"
   "v_cvt_pk_bf16_f32 v81, v82, v83\n"
   "v_cvt_pk_bf16_f32 v82, v84, v85\n"
   "v_cvt_pk_bf16_f32 v83, v86, v87\n"
   "ds_write_b128 v170, v[80:83] offset:16384\n"
   "v_pk_fma_f32 v[88:89], v[88:89], v[228:229], v[236:237]\n"
   "v_pk_fma_f32 v[90:91], v[90:91], v[230:231], v[238:239]\n"
   "v_pk_fma_f32 v[92:93], v[92:93], v[232:233], v[240:241]\n"
   "v_pk_fma_f32 v[94:95], v[94:95], v[234:235], v[242:243]\n"
   "v_cvt_pk_bf16_f32 v88, v88, v89\n"
   "v_cvt_pk_bf16_f32 v89, v90, v91\n"
   "v_cvt_pk_bf16_f32 v90, v92, v93\n"
   "v_cvt_pk_bf16_f32 v91, v94, v95\n"
   "ds_write_b128 v171, v[88:91] offset:16384\n"
   "v_pk_fma_f32 v[96:97], v[96:97], v[228:229], v[236:237]\n"
   "v_pk_fma_f32 v[98:99], v[98:99], v[230:231], v[238:239]\n"
   "v_pk_fma_f32 v[100:101], v[100:101], v[232:233], v[240:241]\n"
   "v_pk_fma_f32 v[102:103], v[102:103], v[234:235], v[242:243]\n"
   "v_cvt_pk_bf16_f32 v96, v96, v97\n"
   "v_cvt_pk_bf16_f32 v97, v98, v99\n"
   "v_cvt_pk_bf16_f32 v98, v100, v101\n"
   "v_cvt_pk_bf16_f32 v99, v102, v103\n"
   "ds_write_b128 v168, v[96:99] offset:32768\n"
   "v_pk_fma_f32 v[104:105], v[104:105], v[228:229], v[236:237]\n"
   "v_pk_fma_f32 v[106:107], v[106:107], v[230:231], v[238:239]\n"
   "v_pk_fma_f32 v[108:109], v[108:109], v[232:233], v[240:241]\n"
   "v_pk_fma_f32 v[110:111], v[110:111], v[234:235], v[242:243]\n"
   "v_cvt_pk_bf16_f32 v104, v104, v105\n"
   "v_cvt_pk_bf16_f32 v105, v106, v107\n"
   "v_cvt_pk_bf16_f32 v106, v108, v109\n"
   "v_cvt_pk_bf16_f32 v107, v110, v111\n"
   "ds_write_b128 v169, v[104:107] offset:32768\n"
   "v_pk_fma_f32 v[112:113], v[112:113], v[228:229], v[236:237]\n"
   "v_pk_fma_f32 v[114:115], v[114:115], v[230:231], v[238:239]\n"
   "v_pk_fma_f32 v[116:117], v[116:117], v[232:233], v[240:241]\n"
   "v_pk_fma_f32 v[118:119], v[118:119], v[234:235], v[242:243]\n"
   "v_cvt_pk_bf16_f32 v112, v112, v113\n"
   "v_cvt_pk_bf16_f32 v113, v114, v115\n"
   "v_cvt_pk_bf16_f32 v114, v116, v117\n"
   "v_cvt_pk_bf16_f32 v115, v118, v119\n"
   "ds_write_b128 v170, v[112:115] offset:32768\n"
   "v_pk_fma_f32 v[120:121], v[120:121], v[228:229], v[236:237]\n"
   "v_pk_fma_f32 v[122:123], v[122:123], v[230:231], v[238:239]\n"
   "v_pk_fma_f32 v[124:125], v[124:125], v[232:233], v[240:241]\n"
   "v_pk_fma_f32 v[126:127], v[126:127], v[234:235], v[242:243]\n"
   "v_cvt_pk_bf16_f32 v120, v120, v121\n"
   "v_cvt_pk_bf16_f32 v121, v122, v123\n"
   "v_cvt_pk_bf16_f32 v122, v124, v125\n"
   "v_cvt_pk_bf16_f32 v123, v126, v127\n"
   "ds_write_b128 v171, v[120:123] offset:32768\n"
   "v_pk_fma_f32 v[128:129], v[128:129], v[228:229], v[236:237]\n"
   "v_pk_fma_f32 v[130:131], v[130:131], v[230:231], v[238:239]\n"
   "v_pk_fma_f32 v[132:133], v[132:133], v[232:233], v[240:241]\n"
   "v_pk_fma_f32 v[134:135], v[134:135], v[234:235], v[242:243]\n"
   "v_cvt_pk_bf16_f32 v128, v128, v129\n"
   "v_cvt_pk_bf16_f32 v129, v130, v131\n"
   "v_cvt_pk_bf16_f32 v130, v132, v133\n"
   "v_cvt_pk_bf16_f32 v131, v134, v135\n"
   "ds_write_b128 v168, v[128:131] offset:49152\n"
   "v_pk_fma_f32 v[136:137], v[136:137], v[228:229], v[236:237]\n"
   "v_pk_fma_f32 v[138:139], v[138:139], v[230:231], v[238:239]\n"
   "v_pk_fma_f32 v[140:141], v[140:141], v[232:233], v[240:241]\n"
   "v_pk_fma_f32 v[142:143], v[142:143], v[234:235], v[242:243]\n"
   "v_cvt_pk_bf16_f32 v136, v136, v137\n"
   "v_cvt_pk_bf16_f32 v137, v138, v139\n"
   "v_cvt_pk_bf16_f32 v138, v140, v141\n"
   "v_cvt_pk_bf16_f32 v139, v142, v143\n"
   "ds_write_b128 v169, v[136:139] offset:49152\n"
   "s_add_u32 m0, s80, 0x22000\n"
   "s_add_u32 s58, s76, 0x30000\n"
   "buffer_load_dwordx4 v161, s[12:15], s58 offen lds\n"
   "buffer_load_dwordx4 v161, s[12:15], s58 offen offset:1024 lds\n"
   "buffer_load_dwordx4 v161, s[12:15], s58 offen offset:2048 lds\n"
   "v_pk_fma_f32 v[144:145], v[144:145], v[228:229], v[236:237]\n"
   "v_pk_fma_f32 v[146:147], v[146:147], v[230:231], v[238:239]\n"
   "v_pk_fma_f32 v[148:149], v[148:149], v[232:233], v[240:241]\n"
   "v_pk_fma_f32 v[150:151], v[150:151], v[234:235], v[242:243]\n"
   "v_cvt_pk_bf16_f32 v144, v144, v145\n"
   "v_cvt_pk_bf16_f32 v145, v146, v147\n"
   "v_cvt_pk_bf16_f32 v146, v148, v149\n"
   "v_cvt_pk_bf16_f32 v147, v150, v151\n"
   "ds_write_b128 v170, v[144:147] offset:49152\n"
   "s_add_u32 m0, s80, 0x25000\n"
   "s_add_u32 s58, s76, 0x48000\n"
   "buffer_load_dwordx4 v161, s[12:15], s58 offen lds\n"
   "buffer_load_dwordx4 v161, s[12:15], s58 offen offset:1024 lds\n"
   "buffer_load_dwordx4 v161, s[12:15], s58 offen offset:2048 lds\n"
   "v_pk_fma_f32 v[152:153], v[152:153], v[228:229], v[236:237]\n"
   "v_pk_fma_f32 v[154:155], v[154:155], v[230:231], v[238:239]\n"
   "v_pk_fma_f32 v[156:157], v[156:157], v[232:233], v[240:241]\n"
   "v_pk_fma_f32 v[158:159], v[158:159], v[234:235], v[242:243]\n"
   "v_cvt_pk_bf16_f32 v152, v152, v153\n"
   "v_cvt_pk_bf16_f32 v153, v154, v155\n"
   "v_cvt_pk_bf16_f32 v154, v156, v157\n"
   "v_cvt_pk_bf16_f32 v155, v158, v159\n"
   "ds_write_b128 v171, v[152:155] offset:49152\n"
   "s_waitcnt vmcnt(12)\n"
   "s_waitcnt lgkmcnt(0)\n"
   "s_barrier\n"
   "s_nop 0\n"
   "ds_read_b128 v[80:83], v172\n"
   "ds_read_b128 v[84:87], v172 offset:16384\n"
   "ds_read_b128 v[88:91], v172 offset:32768\n"
   "ds_read_b128 v[92:95], v172 offset:49152\n"
   "ds_read_b64_tr_b16 a[4:5], v166 offset:1024\n"
   "ds_read_b64_tr_b16 a[6:7], v166 offset:1536\n"
   "ds_read_b64_tr_b16 a[8:9], v166 offset:2048\n"
   "ds_read_b64_tr_b16 a[10:11], v166 offset:2560\n"
   "ds_read_b64_tr_b16 a[0:1], v166\n"
   "ds_read_b64_tr_b16 a[2:3], v166 offset:512\n"
   "s_waitcnt lgkmcnt(6)\n"
   "s_waitcnt lgkmcnt(4)\n"
   "v_mfma_f32_16x16x32_bf16 v[32:35], a[4:7], v[80:83], 0\n"
   "s_waitcnt lgkmcnt(2)\n"
   "ds_read_b128 v[96:99], v173 offset:1024\n"
   "v_mfma_f32_16x16x32_bf16 v[48:51], v[80:83], a[8:11], 0\n"
   "s_waitcnt lgkmcnt(1)\n"
   "ds_read_b64_tr_b16 a[16:17], v166 offset:13312\n"
   "v_mfma_f32_16x16x32_bf16 v[64:67], a[0:3], v[80:83], 0\n"
   "ds_read_b64_tr_b16 a[18:19], v166 offset:13824\n"
   "v_mfma_f32_16x16x32_bf16 v[36:39], a[4:7], v[84:87], 0\n"
   "ds_read_b128 v[100:103], v173 offset:17408\n"
   "ds_read_b64_tr_b16 a[20:21], v166 offset:14336\n"
   "v_mfma_f32_16x16x32_bf16 v[52:55], v[84:87], a[8:11], 0\n"
   "ds_read_b128 v[104:107], v173 offset:33792\n"
   "ds_read_b64_tr_b16 a[22:23], v166 offset:14848\n"
   "v_mfma_f32_16x16x32_bf16 v[68:71], a[0:3], v[84:87], 0\n"
   "ds_read_b128 v[108:111], v173 offset:50176\n"
   "ds_read_b64_tr_b16 a[12:13], v166 offset:12288\n"
   "v_mfma_f32_16x16x32_bf16 v[40:43], a[4:7], v[88:91], 0\n"
   "ds_read_b64_tr_b16 a[14:15], v166 offset:12800\n"
   "v_mfma_f32_16x16x32_bf16 v[56:59], v[88:91], a[8:11], 0\n"
   "ds_read_b64_tr_b16 a[28:29], v166 offset:25600\n"
   "ds_read_b64_tr_b16 a[30:31], v166 offset:26112\n"
   "v_mfma_f32_16x16x32_bf16 v[72:75], a[0:3], v[88:91], 0\n"
   "ds_read_b64_tr_b16 a[32:33], v166 offset:26624\n"
   "v_mfma_f32_16x16x32_bf16 v[44:47], a[4:7], v[92:95], 0\n"
   "ds_read_b64_tr_b16 a[34:35], v166 offset:27136\n"
   "v_mfma_f32_16x16x32_bf16 v[60:63], v[92:95], a[8:11], 0\n"
   "ds_read_b64_tr_b16 a[24:25], v166 offset:24576\n"
   "v_mfma_f32_16x16x32_bf16 v[76:79], a[0:3], v[92:95], 0\n"
   "s_waitcnt lgkmcnt(7)\n"
   "ds_read_b64_tr_b16 a[26:27], v166 offset:25088\n"
   "v_mfma_f32_16x16x32_bf16 v[32:35], a[16:19], v[96:99], v[32:35]\n"
   "ds_read_b128 v[80:83], v172 offset:2048\n"
   "ds_read_b64_tr_b16 a[40:41], v166 offset:37888\n"
   "ds_read_b64_tr_b16 a[42:43], v166 offset:38400\n"
   "v_mfma_f32_16x16x32_bf16 v[48:51], v[96:99], a[20:23], v[48:51]\n"
   "s_waitcnt lgkmcnt(9)\n"
   "ds_read_b128 v[84:87], v172 offset:18432\n"
   "ds_read_b64_tr_b16 a[44:45], v166 offset:38912\n"
   "v_mfma_f32_16x16x32_bf16 v[64:67], a[12:15], v[96:99], v[64:67]\n"
   "ds_read_b64_tr_b16 a[46:47], v166 offset:39424\n"
   "v_mfma_f32_16x16x32_bf16 v[36:39], a[16:19], v[100:103], v[36:39]\n"
   "ds_read_b128 v[88:91], v172 offset:34816\n"
   "ds_read_b64_tr_b16 a[36:37], v166 offset:36864\n"
   "v_mfma_f32_16x16x32_bf16 v[52:55], v[100:103], a[20:23], v[52:55]\n"
   "ds_read_b64_tr_b16 a[38:39], v166 offset:37376\n"
   "v_mfma_f32_16x16x32_bf16 v[68:71], a[12:15], v[100:103], v[68:71]\n"
   "ds_read_b128 v[92:95], v172 offset:51200\n"
   "ds_read_b64_tr_b16 a[52:53], v167 offset:1024\n"
   "ds_read_b64_tr_b16 a[54:55], v167 offset:1536\n"
   "v_mfma_f32_16x16x32_bf16 v[40:43], a[16:19], v[104:107], v[40:43]\n"
   "ds_read_b64_tr_b16 a[56:57], v167 offset:2048\n"
   "v_mfma_f32_16x16x32_bf16 v[56:59], v[104:107], a[20:23], v[56:59]\n"
   "ds_read_b64_tr_b16 a[58:59], v167 offset:2560\n"
   "v_mfma_f32_16x16x32_bf16 v[72:75], a[12:15], v[104:107], v[72:75]\n"
   "ds_read_b64_tr_b16 a[48:49], v167\n"
   "v_mfma_f32_16x16x32_bf16 v[44:47], a[16:19], v[108:111], v[44:47]\n"
   "ds_read_b64_tr_b16 a[50:51], v167 offset:512\n"
   "v_mfma_f32_16x16x32_bf16 v[60:63], v[108:111], a[20:23], v[60:63]\n"
   "v_mfma_f32_16x16x32_bf16 v[76:79], a[12:15], v[108:111], v[76:79]\n"
   "s_waitcnt vmcnt(9)\n"
   "s_waitcnt lgkmcnt(15)\n"
   "s_barrier\n"
   "s_waitcnt lgkmcnt(6)\n"
   "v_mfma_f32_16x16x32_bf16 v[32:35], a[28:31], v[80:83], v[32:35]\n"
   "s_add_u32 m0, s80, 0x10000\n"
   "s_add_u32 s58, s77, 0\n"
   "ds_read_b128 v[96:99], v173 offset:3072\n"
   "v_mfma_f32_16x16x32_bf16 v[48:51], v[80:83], a[32:35], v[48:51]\n"
   "buffer_load_dwordx4 v161, s[12:15], s58 offen lds\n"
   "v_mfma_f32_16x16x32_bf16 v[64:67], a[24:27], v[80:83], v[64:67]\n"
   "s_mul_i32 s3, s1, 0x11000\n"
   "s_add_u32 s3, s3, 0xb00000\n"
   "ds_read_b128 v[100:103], v173 offset:19456\n"
   "v_mfma_f32_16x16x32_bf16 v[36:39], a[28:31], v[84:87], v[36:39]\n"
   "ds_read_b128 v[104:107], v173 offset:35840\n"
   "v_mfma_f32_16x16x32_bf16 v[52:55], v[84:87], a[32:35], v[52:55]\n"
   "buffer_load_dwordx4 v161, s[12:15], s58 offen offset:1024 lds\n"
   "v_mfma_f32_16x16x32_bf16 v[68:71], a[24:27], v[84:87], v[68:71]\n"
   "s_lshl_b32 s56, s54, 7\n"
   "ds_read_b64_tr_b16 a[64:65], v167 offset:13312\n"
   "ds_read_b128 v[108:111], v173 offset:52224\n"
   "v_mfma_f32_16x16x32_bf16 v[40:43], a[28:31], v[88:91], v[40:43]\n"
   "s_add_u32 s88, s56, s3\n"
   "v_mfma_f32_16x16x32_bf16 v[56:59], v[88:91], a[32:35], v[56:59]\n"
   "buffer_load_dwordx4 v161, s[12:15], s58 offen offset:2048 lds\n"
   "v_mfma_f32_16x16x32_bf16 v[72:75], a[24:27], v[88:91], v[72:75]\n"
   "s_add_u32 s89, s88, 0x8000\n"
   "s_add_u32 m0, s80, 0x13000\n"
   "ds_read_b64_tr_b16 a[66:67], v167 offset:13824\n"
   "v_mfma_f32_16x16x32_bf16 v[44:47], a[28:31], v[92:95], v[44:47]\n"
   "s_add_u32 s58, s77, 0x18000\n"
   "v_mfma_f32_16x16x32_bf16 v[60:63], v[92:95], a[32:35], v[60:63]\n"
   "s_add_u32 s90, s88, 0x10000\n"
   "ds_read_b64_tr_b16 a[68:69], v167 offset:14336\n"
   "v_mfma_f32_16x16x32_bf16 v[76:79], a[24:27], v[92:95], v[76:79]\n"
   "s_waitcnt lgkmcnt(2)\n"
   "v_mfma_f32_16x16x32_bf16 v[32:35], a[40:43], v[96:99], v[32:35]\n"
   "buffer_load_dwordx4 v161, s[12:15], s58 offen lds\n"
   "v_mfma_f32_16x16x32_bf16 v[48:51], v[96:99], a[44:47], v[48:51]\n"
   "s_mov_b32 s92, 0xaaaaaaaa\n"
   "ds_read_b128 v[80:83], v172 offset:4096\n"
   "v_mfma_f32_16x16x32_bf16 v[64:67], a[36:39], v[96:99], v[64:67]\n"
   "s_mov_b32 s93, 0xaaaaaaaa\n"
   "ds_read_b64_tr_b16 a[70:71], v167 offset:14848\n"
   "v_mfma_f32_16x16x32_bf16 v[36:39], a[40:43], v[100:103], v[36:39]\n"
   "ds_read_b128 v[84:87], v172 offset:20480\n"
   "v_mfma_f32_16x16x32_bf16 v[52:55], v[100:103], a[44:47], v[52:55]\n"
   "ds_read_b128 v[88:91], v172 offset:36864\n"
   "buffer_load_dwordx4 v161, s[12:15], s58 offen offset:1024 lds\n"
   "v_mfma_f32_16x16x32_bf16 v[68:71], a[36:39], v[100:103], v[68:71]\n"
   "s_lshl3_add_u32 s66, s61, s79\n"
   "ds_read_b64_tr_b16 a[60:61], v167 offset:12288\n"
   "ds_read_b128 v[92:95], v172 offset:53248\n"
   "v_mfma_f32_16x16x32_bf16 v[40:43], a[40:43], v[104:107], v[40:43]\n"
   "buffer_load_dwordx4 v161, s[12:15], s58 offen offset:2048 lds\n"
   "v_mfma_f32_16x16x32_bf16 v[56:59], v[104:107], a[44:47], v[56:59]\n"
   "s_lshl_b32 s81, s51, 9\n"
   "s_add_u32 m0, s80, 0x16000\n"
   "ds_read_b64_tr_b16 a[62:63], v167 offset:12800\n"
   "v_mfma_f32_16x16x32_bf16 v[72:75], a[36:39], v[104:107], v[72:75]\n"
   "s_add_u32 s58, s77, 0x30000\n"
   "s_lshr_b32 s56, s54, 1\n"
   "v_mfma_f32_16x16x32_bf16 v[44:47], a[40:43], v[108:111], v[44:47]\n"
   "buffer_load_dwordx4 v161, s[12:15], s58 offen lds\n"
   "v_mfma_f32_16x16x32_bf16 v[60:63], v[108:111], a[44:47], v[60:63]\n"
   "s_cmp_eq_u32 s54, 1\n"
   "s_cselect_b32 s56, 16, s56\n"
   "s_cselect_b32 s57, 0, s54\n"
   "v_mfma_f32_16x16x32_bf16 v[76:79], a[36:39], v[108:111], v[76:79]\n"
   "s_waitcnt vmcnt(7)\n"
   "s_barrier\n"
   "s_waitcnt lgkmcnt(1)\n"
   "v_mfma_f32_16x16x32_bf16 v[32:35], a[52:55], v[80:83], v[32:35]\n"
   "s_lshl_b32 s71, s56, 12\n"
   "ds_read_b128 v[96:99], v173 offset:5120\n"
   "v_mfma_f32_16x16x32_bf16 v[48:51], v[80:83], a[56:59], v[48:51]\n"
   "buffer_load_dwordx4 v161, s[12:15], s58 offen offset:1024 lds\n"
   "v_mfma_f32_16x16x32_bf16 v[64:67], a[48:51], v[80:83], v[64:67]\n"
   "s_add_u32 s71, s71, s3\n"
   "ds_read_b128 v[100:103], v173 offset:21504\n"
   "v_mfma_f32_16x16x32_bf16 v[36:39], a[52:55], v[84:87], v[36:39]\n"
   "ds_read_b64_tr_b16 a[76:77], v167 offset:25600\n"
   "v_mfma_f32_16x16x32_bf16 v[52:55], v[84:87], a[56:59], v[52:55]\n"
   "s_mul_i32 s87, s1, 0x880\n"
   "ds_read_b128 v[104:107], v173 offset:37888\n"
   "v_mfma_f32_16x16x32_bf16 v[68:71], a[48:51], v[84:87], v[68:71]\n"
   "ds_read_b128 v[108:111], v173 offset:54272\n"
   "buffer_load_dwordx4 v161, s[12:15], s58 offen offset:2048 lds\n"
   "v_mfma_f32_16x16x32_bf16 v[40:43], a[52:55], v[88:91], v[40:43]\n"
   "s_add_u32 m0, s80, 0x19000\n"
   "s_add_u32 s58, s77, 0x48000\n"
   "ds_read_b64_tr_b16 a[78:79], v167 offset:26112\n"
   "v_mfma_f32_16x16x32_bf16 v[56:59], v[88:91], a[56:59], v[56:59]\n"
   "s_add_u32 s87, s87, 0x40000\n"
   "ds_read_b64_tr_b16 a[80:81], v167 offset:26624\n"
   "v_mfma_f32_16x16x32_bf16 v[72:75], a[48:51], v[88:91], v[72:75]\n"
   "buffer_load_dwordx4 v161, s[12:15], s58 offen lds\n"
   "v_mfma_f32_16x16x32_bf16 v[44:47], a[52:55], v[92:95], v[44:47]\n"
   "s_lshl_b32 s56, s56, 7\n"
   "ds_read_b64_tr_b16 a[82:83], v167 offset:27136\n"
   "v_mfma_f32_16x16x32_bf16 v[60:63], v[92:95], a[56:59], v[60:63]\n"
   "s_add_u32 s56, s56, s87\n"
   "ds_read_b64_tr_b16 a[72:73], v167 offset:24576\n"
   "v_mfma_f32_16x16x32_bf16 v[76:79], a[48:51], v[92:95], v[76:79]\n"
   "s_waitcnt lgkmcnt(4)\n"
   "ds_read_b64_tr_b16 a[74:75], v167 offset:25088\n"
   "v_mfma_f32_16x16x32_bf16 v[32:35], a[64:67], v[96:99], v[32:35]\n"
   "buffer_load_dwordx4 v161, s[12:15], s58 offen offset:1024 lds\n"
   "v_mfma_f32_16x16x32_bf16 v[48:51], v[96:99], a[68:71], v[48:51]\n"
   "s_bitcmp1_b32 s57, 0\n"
   "s_cselect_b32 s82, 0x40000000, s56\n"
   "ds_read_b128 v[80:83], v172 offset:6144\n"
   "v_mfma_f32_16x16x32_bf16 v[64:67], a[60:63], v[96:99], v[64:67]\n"
   "ds_read_b64_tr_b16 a[88:89], v167 offset:37888\n"
   "v_mfma_f32_16x16x32_bf16 v[36:39], a[64:67], v[100:103], v[36:39]\n"
   "s_lshl_b32 s56, s2, 7\n"
   "ds_read_b128 v[84:87], v172 offset:22528\n"
   "v_mfma_f32_16x16x32_bf16 v[52:55], v[100:103], a[68:71], v[52:55]\n"
   "ds_read_b64_tr_b16 a[90:91], v167 offset:38400\n"
   "ds_read_b128 v[88:91], v172 offset:38912\n"
   "v_mfma_f32_16x16x32_bf16 v[68:71], a[60:63], v[100:103], v[68:71]\n"
   "ds_read_b128 v[92:95], v172 offset:55296\n"
   "buffer_load_dwordx4 v161, s[12:15], s58 offen offset:2048 lds\n"
   "v_mfma_f32_16x16x32_bf16 v[40:43], a[64:67], v[104:107], v[40:43]\n"
   "s_add_u32 m0, s80, 0x1c000\n"
   "s_add_u32 s58, s78, 0\n"
   "ds_read_b64_tr_b16 a[92:93], v167 offset:38912\n"
   "v_mfma_f32_16x16x32_bf16 v[56:59], v[104:107], a[68:71], v[56:59]\n"
   "s_lshr_b32 s57, s2, 6\n"
   "s_lshl2_add_u32 s57, s57, s51\n"
   "ds_read_b64_tr_b16 a[94:95], v167 offset:39424\n"
   "v_mfma_f32_16x16x32_bf16 v[72:75], a[60:63], v[104:107], v[72:75]\n"
   "ds_read_b64_tr_b16 a[84:85], v167 offset:36864\n"
   "buffer_load_dwordx4 v161, s[12:15], s58 offen lds\n"
   "v_mfma_f32_16x16x32_bf16 v[44:47], a[64:67], v[108:111], v[44:47]\n"
   "s_cmp_eq_u32 s57, 6\n"
   "s_cselect_b32 s91, s56, 0x40000000\n"
   "ds_read_b64_tr_b16 a[86:87], v167 offset:37376\n"
   "v_mfma_f32_16x16x32_bf16 v[60:63], v[108:111], a[68:71], v[60:63]\n"
   "buffer_load_dwordx4 v161, s[12:15], s58 offen offset:1024 lds\n"
   "v_mfma_f32_16x16x32_bf16 v[76:79], a[60:63], v[108:111], v[76:79]\n"
   "s_waitcnt vmcnt(8)\n"
   "s_waitcnt lgkmcnt(10)\n"
   "s_barrier\n"
   "s_waitcnt lgkmcnt(4)\n"
   "v_mfma_f32_16x16x32_bf16 v[32:35], a[76:79], v[80:83], v[32:35]\n"
   "s_add_u32 s73, s73, s67\n"
   "ds_read_b128 v[96:99], v173 offset:7168\n"
   "v_mfma_f32_16x16x32_bf16 v[48:51], v[80:83], a[80:83], v[48:51]\n"
   "ds_read_b64_tr_b16 a[100:101], v166 offset:1024\n"
   "v_mfma_f32_16x16x32_bf16 v[64:67], a[72:75], v[80:83], v[64:67]\n"
   "buffer_load_dwordx4 v161, s[12:15], s58 offen offset:2048 lds\n"
   "v_mfma_f32_16x16x32_bf16 v[36:39], a[76:79], v[84:87], v[36:39]\n"
   "s_add_u32 m0, s80, 0x1f000\n"
   "s_add_u32 s58, s78, 0x18000\n"
   "ds_read_b128 v[100:103], v173 offset:23552\n"
   "v_mfma_f32_16x16x32_bf16 v[52:55], v[84:87], a[80:83], v[52:55]\n"
   "ds_read_b64_tr_b16 a[102:103], v166 offset:1536\n"
   "ds_read_b128 v[104:107], v173 offset:39936\n"
   "v_mfma_f32_16x16x32_bf16 v[68:71], a[72:75], v[84:87], v[68:71]\n"
   "s_lshl_b32 s56, s51, 6\n"
   "ds_read_b64_tr_b16 a[104:105], v166 offset:2048\n"
   "ds_read_b128 v[108:111], v173 offset:56320\n"
   "v_mfma_f32_16x16x32_bf16 v[40:43], a[76:79], v[88:91], v[40:43]\n"
   "buffer_load_dwordx4 v161, s[12:15], s58 offen lds\n"
   "v_mfma_f32_16x16x32_bf16 v[56:59], v[88:91], a[80:83], v[56:59]\n"
   "s_add_u32 s73, s73, s56\n"
   "ds_read_b64_tr_b16 a[106:107], v166 offset:2560\n"
   "v_mfma_f32_16x16x32_bf16 v[72:75], a[72:75], v[88:91], v[72:75]\n"
   "ds_read_b64_tr_b16 a[96:97], v166\n"
   "v_mfma_f32_16x16x32_bf16 v[44:47], a[76:79], v[92:95], v[44:47]\n"
   "buffer_load_dwordx4 v161, s[12:15], s58 offen offset:1024 lds\n"
   "v_mfma_f32_16x16x32_bf16 v[60:63], v[92:95], a[80:83], v[60:63]\n"
   "s_add_u32 s73, s73, 0x1000\n"
   "ds_read_b64_tr_b16 a[98:99], v166 offset:512\n"
   "v_accvgpr_read_b32 v112, a225\n"
   "v_mfma_f32_16x16x32_bf16 v[76:79], a[72:75], v[92:95], v[76:79]\n"
   "s_waitcnt lgkmcnt(3)\n"
   "ds_read_b64_tr_b16 a[112:113], v166 offset:13312\n"
   "v_accvgpr_read_b32 v113, a232\n"
   "v_mfma_f32_16x16x32_bf16 v[32:35], a[88:91], v[96:99], v[32:35]\n"
   "buffer_load_dwordx4 v161, s[12:15], s58 offen offset:2048 lds\n"
   "v_mfma_f32_16x16x32_bf16 v[48:51], v[96:99], a[92:95], v[48:51]\n"
   "s_lshr_b32 s56, s51, 1\n"
   "s_add_u32 m0, s80, 0x22000\n"
   "ds_read_b64_tr_b16 a[114:115], v166 offset:13824\n"
   "v_mfma_f32_16x16x32_bf16 v[64:67], a[84:87], v[96:99], v[64:67]\n"
   "s_add_u32 s58, s78, 0x30000\n"
   "s_lshl_b32 s60, s51, 5\n"
   "ds_read_b128 v[80:83], v172 offset:8192\n"
   "v_mfma_f32_16x16x32_bf16 v[36:39], a[88:91], v[100:103], v[36:39]\n"
   "ds_read_b64_tr_b16 a[116:117], v166 offset:14336\n"
   "ds_read_b128 v[84:87], v172 offset:24576\n"
   "v_mfma_f32_16x16x32_bf16 v[52:55], v[100:103], a[92:95], v[52:55]\n"
   "ds_read_b64_tr_b16 a[118:119], v166 offset:14848\n"
   "ds_read_b128 v[88:91], v172 offset:40960\n"
   "buffer_load_dwordx4 v161, s[12:15], s58 offen lds\n"
   "v_mfma_f32_16x16x32_bf16 v[68:71], a[84:87], v[100:103], v[68:71]\n"
   "s_mul_i32 s56, s56, 0x4000\n"
   "ds_read_b128 v[92:95], v172 offset:57344\n"
   "v_xor_b32_e64 v113, s60, v113\n"
   "v_add_u32 v113, 0x10000, v113\n"
   "v_mfma_f32_16x16x32_bf16 v[40:43], a[88:91], v[104:107], v[40:43]\n"
   "ds_read_b64_tr_b16 a[108:109], v166 offset:12288\n"
   "ds_read_b64_tr_b16 a[110:111], v166 offset:12800\n"
   "v_accvgpr_read_b32 v175, a220\n"
   "v_mfma_f32_16x16x32_bf16 v[56:59], v[104:107], a[92:95], v[56:59]\n"
   "buffer_load_dwordx4 v161, s[12:15], s58 offen offset:1024 lds\n"
   "v_mfma_f32_16x16x32_bf16 v[72:75], a[84:87], v[104:107], v[72:75]\n"
   "s_lshr_b32 s67, s61, 1\n"
   "v_xor_b32 v184, 16, v175\n"
   "v_accvgpr_read_b32 v186, a221\n"
   "v_mfma_f32_16x16x32_bf16 v[44:47], a[88:91], v[108:111], v[44:47]\n"
   "s_add_u32 s67, s67, s56\n"
   "v_xor_b32_e64 v176, 32, v186\n"
   "v_accvgpr_read_b32 v177, a222\n"
   "v_mfma_f32_16x16x32_bf16 v[60:63], v[108:111], a[92:95], v[60:63]\n"
   "buffer_load_dwordx4 v161, s[12:15], s58 offen offset:2048 lds\n"
   "v_mfma_f32_16x16x32_bf16 v[76:79], a[84:87], v[108:111], v[76:79]\n"
   "v_accvgpr_read_b32 v179, a223\n"
   "s_waitcnt vmcnt(9)\n"
   "s_waitcnt lgkmcnt(0)\n"
   "s_barrier\n"
   "v_mfma_f32_16x16x32_bf16 v[32:35], a[100:103], v[80:83], v[32:35]\n"
   "s_add_u32 m0, s80, 0x25000\n"
   "s_add_u32 s58, s78, 0x48000\n"
   "ds_read_b128 v[96:99], v173 offset:9216\n"
   "v_mfma_f32_16x16x32_bf16 v[48:51], v[80:83], a[104:107], v[48:51]\n"
   "ds_read_b64_tr_b16 a[124:125], v166 offset:25600\n"
   "v_mfma_f32_16x16x32_bf16 v[64:67], a[96:99], v[80:83], v[64:67]\n"
   "buffer_load_dwordx4 v161, s[12:15], s58 offen lds\n"
   "v_mfma_f32_16x16x32_bf16 v[36:39], a[100:103], v[84:87], v[36:39]\n"
   "ds_read_b128 v[100:103], v173 offset:25600\n"
   "v_mfma_f32_16x16x32_bf16 v[52:55], v[84:87], a[104:107], v[52:55]\n"
   "ds_read_b64_tr_b16 a[126:127], v166 offset:26112\n"
   "ds_read_b128 v[104:107], v173 offset:41984\n"
   "v_mfma_f32_16x16x32_bf16 v[68:71], a[96:99], v[84:87], v[68:71]\n"
   "ds_read_b64_tr_b16 a[128:129], v166 offset:26624\n"
   "ds_read_b128 v[108:111], v173 offset:58368\n"
   "v_mfma_f32_16x16x32_bf16 v[40:43], a[100:103], v[88:91], v[40:43]\n"
   "buffer_load_dwordx4 v161, s[12:15], s58 offen offset:1024 lds\n"
   "v_mfma_f32_16x16x32_bf16 v[56:59], v[88:91], a[104:107], v[56:59]\n"
   "ds_read_b64_tr_b16 a[130:131], v166 offset:27136\n"
   "v_mfma_f32_16x16x32_bf16 v[72:75], a[96:99], v[88:91], v[72:75]\n"
   "ds_read_b64_tr_b16 a[120:121], v166 offset:24576\n"
   "v_mfma_f32_16x16x32_bf16 v[44:47], a[100:103], v[92:95], v[44:47]\n"
   "ds_read_b64_tr_b16 a[122:123], v166 offset:25088\n"
   "v_mfma_f32_16x16x32_bf16 v[60:63], v[92:95], a[104:107], v[60:63]\n"
   "buffer_load_dwordx4 v161, s[12:15], s58 offen offset:2048 lds\n"
   "v_mfma_f32_16x16x32_bf16 v[76:79], a[96:99], v[92:95], v[76:79]\n"
   "s_waitcnt lgkmcnt(3)\n"
   "ds_read_b64_tr_b16 a[136:137], v166 offset:37888\n"
   "v_accvgpr_read_b32 v11, a224\n"
   "v_mfma_f32_16x16x32_bf16 v[32:35], a[112:115], v[96:99], v[32:35]\n"
   "s_add_u32 m0, s67, 0x10000\n"
   "s_add_u32 s58, s74, 0\n"
   "ds_read_b64_tr_b16 a[138:139], v166 offset:38400\n"
   "v_mfma_f32_16x16x32_bf16 v[48:51], v[96:99], a[116:119], v[48:51]\n"
   "ds_read_b128 v[80:83], v172 offset:10240\n"
   "v_add_u32 v12, 0x1c00, v11\n"
   "v_mfma_f32_16x16x32_bf16 v[64:67], a[108:111], v[96:99], v[64:67]\n"
   "ds_read_b64_tr_b16 a[140:141], v166 offset:38912\n"
   "v_add_u32 v13, 0x3800, v11\n"
   "v_mfma_f32_16x16x32_bf16 v[36:39], a[112:115], v[100:103], v[36:39]\n"
   "ds_read_b128 v[84:87], v172 offset:26624\n"
   "ds_read_b64_tr_b16 a[142:143], v166 offset:39424\n"
   "buffer_load_dwordx4 v112, s[36:39], s58 offen lds\n"
   "v_mfma_f32_16x16x32_bf16 v[52:55], v[100:103], a[116:119], v[52:55]\n"
   "s_add_u32 m0, s67, 0x10400\n"
   "s_add_u32 s58, s74, 0xc000\n"
   "v_add_u32 v174, 0x5400, v11\n"
   "v_mfma_f32_16x16x32_bf16 v[68:71], a[108:111], v[100:103], v[68:71]\n"
   "ds_read_b128 v[88:91], v172 offset:43008\n"
   "ds_read_b64_tr_b16 a[132:133], v166 offset:36864\n"
   "ds_read_b128 v[92:95], v172 offset:59392\n"
   "ds_read_b64_tr_b16 a[134:135], v166 offset:37376\n"
   "v_mfma_f32_16x16x32_bf16 v[40:43], a[112:115], v[104:107], v[40:43]\n"
   "v_accvgpr_read_b32 v114, a229\n"
   "v_add_u32 v117, 0xc000, v114\n"
   "v_mfma_f32_16x16x32_bf16 v[56:59], v[104:107], a[116:119], v[56:59]\n"
   "buffer_load_dwordx4 v112, s[36:39], s58 offen lds\n"
   "v_mfma_f32_16x16x32_bf16 v[72:75], a[108:111], v[104:107], v[72:75]\n"
   "s_sub_u32 s59, 0x180, s20\n"
   "s_and_b32 s59, s59, 0x380\n"
   "v_accvgpr_read_b32 v115, a230\n"
   "v_mfma_f32_16x16x32_bf16 v[44:47], a[112:115], v[108:111], v[44:47]\n"
   "v_add_u32 v118, 0xc000, v115\n"
   "v_accvgpr_read_b32 v116, a231\n"
   "v_mfma_f32_16x16x32_bf16 v[60:63], v[108:111], a[116:119], v[60:63]\n"
   "buffer_load_dword v5, v188, s[20:23], s59 offen\n"
   "v_mfma_f32_16x16x32_bf16 v[76:79], a[108:111], v[108:111], v[76:79]\n"
   "v_add_u32 v119, 0xc000, v116\n"
   "s_waitcnt vmcnt(6)\n"
   "s_waitcnt lgkmcnt(0)\n"
   "s_barrier\n"
   "v_mfma_f32_16x16x32_bf16 v[32:35], a[124:127], v[80:83], v[32:35]\n"
   "s_add_u32 m0, s80, 0x16000\n"
   "s_add_u32 s58, s83, 0\n"
   "ds_read_b128 v[96:99], v173 offset:11264\n"
   "v_mfma_f32_16x16x32_bf16 v[48:51], v[80:83], a[128:131], v[48:51]\n"
   "ds_read_b64_tr_b16 a[148:149], v167 offset:1024\n"
   "v_mfma_f32_16x16x32_bf16 v[64:67], a[120:123], v[80:83], v[64:67]\n"
   "buffer_load_dwordx4 v189, s[24:27], s58 offen lds\n"
   "v_mfma_f32_16x16x32_bf16 v[36:39], a[124:127], v[84:87], v[36:39]\n"
   "ds_read_b64_tr_b16 a[150:151], v167 offset:1536\n"
   "ds_read_b128 v[100:103], v173 offset:27648\n"
   "v_mfma_f32_16x16x32_bf16 v[52:55], v[84:87], a[128:131], v[52:55]\n"
   "ds_read_b64_tr_b16 a[152:153], v167 offset:2048\n"
   "ds_read_b128 v[104:107], v173 offset:44032\n"
   "v_mfma_f32_16x16x32_bf16 v[68:71], a[120:123], v[84:87], v[68:71]\n"
   "ds_read_b64_tr_b16 a[154:155], v167 offset:2560\n"
   "ds_read_b128 v[108:111], v173 offset:60416\n"
   "v_mfma_f32_16x16x32_bf16 v[40:43], a[124:127], v[88:91], v[40:43]\n"
   "ds_read_b64_tr_b16 a[144:145], v167\n"
   "buffer_load_dwordx4 v190, s[24:27], s58 offen offset:1024 lds\n"
   "v_mfma_f32_16x16x32_bf16 v[56:59], v[88:91], a[128:131], v[56:59]\n"
   "ds_read_b64_tr_b16 a[146:147], v167 offset:512\n"
   "v_mfma_f32_16x16x32_bf16 v[72:75], a[120:123], v[88:91], v[72:75]\n"
   "ds_read_b64_tr_b16 a[160:161], v167 offset:13312\n"
   "v_mfma_f32_16x16x32_bf16 v[44:47], a[124:127], v[92:95], v[44:47]\n"
   "ds_read_b64_tr_b16 a[162:163], v167 offset:13824\n"
   "v_mfma_f32_16x16x32_bf16 v[60:63], v[92:95], a[128:131], v[60:63]\n"
   "ds_read_b64_tr_b16 a[164:165], v167 offset:14336\n"
   "buffer_load_dwordx4 v191, s[24:27], s58 offen offset:2048 lds\n"
   "v_mfma_f32_16x16x32_bf16 v[76:79], a[120:123], v[92:95], v[76:79]\n"
   "s_waitcnt lgkmcnt(5)\n"
   "s_add_u32 m0, s80, 0x19000\n"
   "ds_read_b64_tr_b16 a[166:167], v167 offset:14848\n"
   "v_mfma_f32_16x16x32_bf16 v[32:35], a[136:139], v[96:99], v[32:35]\n"
   "s_add_u32 s58, s83, 0x15800\n"
   "ds_read_b64_tr_b16 a[156:157], v167 offset:12288\n"
   "v_mfma_f32_16x16x32_bf16 v[48:51], v[96:99], a[140:143], v[48:51]\n"
   "ds_read_b128 v[80:83], v172 offset:12288\n"
   "ds_read_b64_tr_b16 a[158:159], v167 offset:12800\n"
   "v_mfma_f32_16x16x32_bf16 v[64:67], a[132:135], v[96:99], v[64:67]\n"
   "ds_read_b64_tr_b16 a[172:173], v167 offset:25600\n"
   "buffer_load_dwordx4 v189, s[24:27], s58 offen lds\n"
   "v_mfma_f32_16x16x32_bf16 v[36:39], a[136:139], v[100:103], v[36:39]\n"
   "ds_read_b128 v[84:87], v172 offset:28672\n"
   "ds_read_b64_tr_b16 a[174:175], v167 offset:26112\n"
   "v_mfma_f32_16x16x32_bf16 v[52:55], v[100:103], a[140:143], v[52:55]\n"
   "ds_read_b128 v[88:91], v172 offset:45056\n"
   "ds_read_b64_tr_b16 a[176:177], v167 offset:26624\n"
   "v_mfma_f32_16x16x32_bf16 v[68:71], a[132:135], v[100:103], v[68:71]\n"
   "ds_read_b128 v[92:95], v172 offset:61440\n"
   "ds_read_b64_tr_b16 a[178:179], v167 offset:27136\n"
   "buffer_load_dwordx4 v190, s[24:27], s58 offen offset:1024 lds\n"
   "v_mfma_f32_16x16x32_bf16 v[40:43], a[136:139], v[104:107], v[40:43]\n"
   "ds_read_b64_tr_b16 a[168:169], v167 offset:24576\n"
   "v_mfma_f32_16x16x32_bf16 v[56:59], v[104:107], a[140:143], v[56:59]\n"
   "ds_read_b64_tr_b16 a[170:171], v167 offset:25088\n"
   "v_mfma_f32_16x16x32_bf16 v[72:75], a[132:135], v[104:107], v[72:75]\n"
   "buffer_load_dwordx4 v191, s[24:27], s58 offen offset:2048 lds\n"
   "v_mfma_f32_16x16x32_bf16 v[44:47], a[136:139], v[108:111], v[44:47]\n"
   "v_mfma_f32_16x16x32_bf16 v[60:63], v[108:111], a[140:143], v[60:63]\n"
   "v_mfma_f32_16x16x32_bf16 v[76:79], a[132:135], v[108:111], v[76:79]\n"
   "s_waitcnt vmcnt(9)\n"
   "s_waitcnt lgkmcnt(0)\n"
   "s_barrier\n"
   "v_mfma_f32_16x16x32_bf16 v[32:35], a[148:151], v[80:83], v[32:35]\n"
   "s_add_u32 m0, s80, 0x1c000\n"
   "s_add_u32 s58, s83, 0x2b000\n"
   "ds_read_b128 v[96:99], v173 offset:13312\n"
   "v_mfma_f32_16x16x32_bf16 v[48:51], v[80:83], a[152:155], v[48:51]\n"
   "ds_read_b64_tr_b16 a[184:185], v167 offset:37888\n"
   "v_mfma_f32_16x16x32_bf16 v[64:67], a[144:147], v[80:83], v[64:67]\n"
   "buffer_load_dwordx4 v189, s[24:27], s58 offen lds\n"
   "v_mfma_f32_16x16x32_bf16 v[36:39], a[148:151], v[84:87], v[36:39]\n"
   "ds_read_b128 v[100:103], v173 offset:29696\n"
   "v_mfma_f32_16x16x32_bf16 v[52:55], v[84:87], a[152:155], v[52:55]\n"
   "ds_read_b64_tr_b16 a[186:187], v167 offset:38400\n"
   "ds_read_b128 v[104:107], v173 offset:46080\n"
   "v_mfma_f32_16x16x32_bf16 v[68:71], a[144:147], v[84:87], v[68:71]\n"
   "ds_read_b64_tr_b16 a[188:189], v167 offset:38912\n"
   "ds_read_b128 v[108:111], v173 offset:62464\n"
   "v_mfma_f32_16x16x32_bf16 v[40:43], a[148:151], v[88:91], v[40:43]\n"
   "buffer_load_dwordx4 v190, s[24:27], s58 offen offset:1024 lds\n"
   "v_mfma_f32_16x16x32_bf16 v[56:59], v[88:91], a[152:155], v[56:59]\n"
   "ds_read_b64_tr_b16 a[190:191], v167 offset:39424\n"
   "v_mfma_f32_16x16x32_bf16 v[72:75], a[144:147], v[88:91], v[72:75]\n"
   "ds_read_b64_tr_b16 a[180:181], v167 offset:36864\n"
   "v_mfma_f32_16x16x32_bf16 v[44:47], a[148:151], v[92:95], v[44:47]\n"
   "ds_read_b64_tr_b16 a[182:183], v167 offset:37376\n"
   "buffer_load_dwordx4 v191, s[24:27], s58 offen offset:2048 lds\n"
   "v_mfma_f32_16x16x32_bf16 v[60:63], v[92:95], a[152:155], v[60:63]\n"
   "s_add_u32 m0, s80, 0x1f000\n"
   "s_add_u32 s58, s83, 0x40800\n"
   "v_mfma_f32_16x16x32_bf16 v[76:79], a[144:147], v[92:95], v[76:79]\n"
   "s_waitcnt lgkmcnt(3)\n"
   "v_mfma_f32_16x16x32_bf16 v[32:35], a[160:163], v[96:99], v[32:35]\n"
   "ds_read_b128 v[80:83], v172 offset:14336\n"
   "v_mfma_f32_16x16x32_bf16 v[48:51], v[96:99], a[164:167], v[48:51]\n"
   "buffer_load_dwordx4 v189, s[24:27], s58 offen lds\n"
   "v_mfma_f32_16x16x32_bf16 v[64:67], a[156:159], v[96:99], v[64:67]\n"
   "ds_read_b128 v[84:87], v172 offset:30720\n"
   "v_mfma_f32_16x16x32_bf16 v[36:39], a[160:163], v[100:103], v[36:39]\n"
   "ds_read_b128 v[88:91], v172 offset:47104\n"
   "v_mfma_f32_16x16x32_bf16 v[52:55], v[100:103], a[164:167], v[52:55]\n"
   "buffer_load_dwordx4 v190, s[24:27], s58 offen offset:1024 lds\n"
   "v_mfma_f32_16x16x32_bf16 v[68:71], a[156:159], v[100:103], v[68:71]\n"
   "ds_read_b128 v[92:95], v172 offset:63488\n"
   "v_mfma_f32_16x16x32_bf16 v[40:43], a[160:163], v[104:107], v[40:43]\n"
   "v_mfma_f32_16x16x32_bf16 v[56:59], v[104:107], a[164:167], v[56:59]\n"
   "v_mfma_f32_16x16x32_bf16 v[72:75], a[156:159], v[104:107], v[72:75]\n"
   "buffer_load_dwordx4 v191, s[24:27], s58 offen offset:2048 lds\n"
   "v_mfma_f32_16x16x32_bf16 v[44:47], a[160:163], v[108:111], v[44:47]\n"
   "v_mfma_f32_16x16x32_bf16 v[60:63], v[108:111], a[164:167], v[60:63]\n"
   "v_mfma_f32_16x16x32_bf16 v[76:79], a[156:159], v[108:111], v[76:79]\n"
   "s_waitcnt vmcnt(6)\n"
   "s_waitcnt lgkmcnt(4)\n"
   "s_barrier\n"
   "s_waitcnt lgkmcnt(0)\n"
   "v_mfma_f32_16x16x32_bf16 v[32:35], a[172:175], v[80:83], v[32:35]\n"
   "s_add_u32 m0, s80, 0x22000\n"
   "s_add_u32 s58, s83, 0x56000\n"
   "ds_read_b128 v[96:99], v173 offset:15360\n"
   "v_mfma_f32_16x16x32_bf16 v[48:51], v[80:83], a[176:179], v[48:51]\n"
   "ds_read_b64_tr_b16 v[16:17], v113\n"
   "ds_read_b64_tr_b16 v[18:19], v113 offset:1024\n"
   "v_mfma_f32_16x16x32_bf16 v[64:67], a[168:171], v[80:83], v[64:67]\n"
   "ds_read_b64_tr_b16 v[20:21], v113 offset:20480\n"
   "ds_read_b64_tr_b16 a[0:1], v114 offset:24576\n"
   "buffer_load_dwordx4 v189, s[24:27], s58 offen lds\n"
   "v_mfma_f32_16x16x32_bf16 v[36:39], a[172:175], v[84:87], v[36:39]\n"
   "ds_read_b128 v[100:103], v173 offset:31744\n"
   "ds_read_b64_tr_b16 a[2:3], v114 offset:30720\n"
   "ds_read_b64_tr_b16 v[22:23], v113 offset:21504\n"
   "v_mfma_f32_16x16x32_bf16 v[52:55], v[84:87], a[176:179], v[52:55]\n"
   "ds_read_b128 v[104:107], v173 offset:48128\n"
   "ds_read_b64_tr_b16 a[4:5], v115 offset:24576\n"
   "ds_read_b64_tr_b16 a[6:7], v115 offset:30720\n"
   "v_mfma_f32_16x16x32_bf16 v[68:71], a[168:171], v[84:87], v[68:71]\n"
   "ds_read_b128 v[108:111], v173 offset:64512\n"
   "ds_read_b64_tr_b16 a[8:9], v116 offset:24576\n"
   "ds_read_b64_tr_b16 a[10:11], v116 offset:30720\n"
   "v_mfma_f32_16x16x32_bf16 v[40:43], a[172:175], v[88:91], v[40:43]\n"
   "ds_read_b64_tr_b16 a[12:13], v114 offset:36864\n"
   "buffer_load_dwordx4 v190, s[24:27], s58 offen offset:1024 lds\n"
   "v_mfma_f32_16x16x32_bf16 v[56:59], v[88:91], a[176:179], v[56:59]\n"
   "ds_read_b64_tr_b16 a[14:15], v114 offset:43008\n"
   "v_mfma_f32_16x16x32_bf16 v[72:75], a[168:171], v[88:91], v[72:75]\n"
   "ds_read_b64_tr_b16 a[16:17], v115 offset:36864\n"
   "ds_read_b64_tr_b16 a[18:19], v115 offset:43008\n"
   "v_mfma_f32_16x16x32_bf16 v[44:47], a[172:175], v[92:95], v[44:47]\n"
   "ds_read_b64_tr_b16 a[20:21], v116 offset:36864\n"
   "buffer_load_dwordx4 v191, s[24:27], s58 offen offset:2048 lds\n"
   "v_mfma_f32_16x16x32_bf16 v[60:63], v[92:95], a[176:179], v[60:63]\n"
   "ds_read_b64_tr_b16 a[22:23], v116 offset:43008\n"
   "v_mfma_f32_16x16x32_bf16 v[76:79], a[168:171], v[92:95], v[76:79]\n"
   "s_waitcnt lgkmcnt(15)\n"
   "v_mfma_f32_16x16x32_bf16 v[24:27], v[16:19], a[212:215], 0\n"
   "s_waitcnt lgkmcnt(12)\n"
   "v_mfma_f32_16x16x32_bf16 v[24:27], v[20:23], a[216:219], v[24:27]\n"
   "s_memrealtime s[68:69]\n"
   "s_waitcnt lgkmcnt(0)\n"
   "s_barrier\n"
   "v_mfma_f32_16x16x32_bf16 v[32:35], a[184:187], v[96:99], v[32:35]\n"
   "s_add_u32 m0, s80, 0x25000\n"
   "s_add_u32 s58, s83, 0x6b800\n"
   "s_sub_u32 s0, s68, s0\n"
   "v_mfma_f32_16x16x32_bf16 v[48:51], v[96:99], a[188:191], v[48:51]\n"
   "buffer_load_dwordx4 v189, s[24:27], s58 offen lds\n"
   "v_mov_b32 v208, s68\n"
   "v_mov_b32 v209, s0\n"
   "v_mfma_f32_16x16x32_bf16 v[36:39], a[184:187], v[100:103], v[36:39]\n"
   "v_mov_b32 v210, s68\n"
   "v_mov_b32 v211, s0\n"
   "v_lshrrev_b32_e64 v120, 2, v177\n"
   "v_mfma_f32_16x16x32_bf16 v[52:55], v[100:103], a[188:191], v[52:55]\n"
   "buffer_load_dwordx4 v190, s[24:27], s58 offen offset:1024 lds\n"
   "v_bfi_b32 v120, v15, v177, v120\n"
   "s_lshl_b32 s56, s2, 7\n"
   "v_mfma_f32_16x16x32_bf16 v[40:43], a[184:187], v[104:107], v[40:43]\n"
   "s_add_u32 s56, s56, 0x8000\n"
   "s_lshl3_add_u32 s57, s51, s2\n"
   "v_mfma_f32_16x16x32_bf16 v[56:59], v[104:107], a[188:191], v[56:59]\n"
   "s_cmp_lt_u32 s57, 8\n"
   "s_cselect_b32 s56, s56, 0x40000000\n"
   "buffer_store_dwordx4 v[208:211], v120, s[44:47], s56 offen sc1\n"
   "v_bfi_b32 v24, v15, s86, v24\n"
   "v_mfma_f32_16x16x32_bf16 v[44:47], a[184:187], v[108:111], v[44:47]\n"
   "buffer_load_dwordx4 v191, s[24:27], s58 offen offset:2048 lds\n"
   "v_mfma_f32_16x16x32_bf16 v[60:63], v[108:111], a[188:191], v[60:63]\n"
   "v_bfi_b32 v26, v15, s86, v26\n"
   "v_mfma_f32_16x16x32_bf16 v[64:67], a[180:183], v[96:99], v[64:67]\n"
   "s_mov_b32 exec_lo, 0x10001\n"
   "s_mov_b32 exec_hi, 0x10001\n"
   "buffer_store_dwordx4 v[24:27], v179, s[44:47], s73 offen sc1\n"
   "s_mov_b64 exec, -1\n"
   "v_mfma_f32_16x16x32_bf16 v[68:71], a[180:183], v[100:103], v[68:71]\n"
   "v_cvt_pk_bf16_f32 v200, v32, v33\n"
   "v_cvt_pk_bf16_f32 v201, v34, v35\n"
   "v_mfma_f32_16x16x32_bf16 v[72:75], a[180:183], v[104:107], v[72:75]\n"
   "v_cvt_pk_bf16_f32 v202, v36, v37\n"
   "v_cvt_pk_bf16_f32 v203, v38, v39\n"
   "v_mfma_f32_16x16x32_bf16 v[76:79], a[180:183], v[108:111], v[76:79]\n"
   "v_cvt_pk_bf16_f32 v204, v40, v41\n"
   "v_cvt_pk_bf16_f32 v205, v42, v43\n"
   ".Lfb_pc13:\n"
   "s_lshr_b32 s60, s2, 3\n"
   "s_lshl_b32 s60, s60, 7\n"
   "s_add_u32 s60, s60, .Lfb_pc13-.Lfb_pc\n"
   "v_lshlrev_b32 v5, 12, v0\n"
   "buffer_load_dword v5, v5, s[4:7], s60 offen\n"
   "v_mfma_f32_16x16x32_bf16 v[124:127], a[192:195], v[200:203], 0\n"
   "v_cvt_pk_bf16_f32 v206, v44, v45\n"
   "v_cvt_pk_bf16_f32 v207, v46, v47\n"
   "v_mfma_f32_16x16x32_bf16 v[128:131], a[196:199], v[200:203], 0\n"
   "v_cvt_pk_bf16_f32 v80, v48, v49\n"
   "v_cvt_pk_bf16_f32 v81, v50, v51\n"
   "v_mfma_f32_16x16x32_bf16 v[132:135], a[200:203], v[200:203], 0\n"
   "v_cvt_pk_bf16_f32 v82, v52, v53\n"
   "v_cvt_pk_bf16_f32 v83, v54, v55\n"
   "v_mfma_f32_16x16x32_bf16 v[136:139], a[204:207], v[200:203], 0\n"
   "v_cvt_pk_bf16_f32 v84, v56, v57\n"
   "v_cvt_pk_bf16_f32 v85, v58, v59\n"
   "v_mfma_f32_16x16x32_bf16 v[140:143], a[192:195], v[204:207], 0\n"
   "v_cvt_pk_bf16_f32 v86, v60, v61\n"
   "v_cvt_pk_bf16_f32 v87, v62, v63\n"
   "v_mfma_f32_16x16x32_bf16 v[144:147], a[196:199], v[204:207], 0\n"
   "v_max3_f32 v122, v124, v125, v126\n"
   "v_max3_f32 v122, v122, v127, v128\n"
   "v_mfma_f32_16x16x32_bf16 v[148:151], a[200:203], v[204:207], 0\n"
   "v_max3_f32 v122, v122, v129, v130\n"
   "v_max3_f32 v122, v122, v131, v132\n"
   "v_mfma_f32_16x16x32_bf16 v[152:155], a[204:207], v[204:207], 0\n"
   "v_max3_f32 v122, v122, v133, v134\n"
   "v_max3_f32 v122, v122, v135, v136\n"
   "v_max3_f32 v122, v122, v137, v138\n"
   "v_max_f32_e64 v122, v122, v139\n"
   "v_mul_f32 v122, 0xbfb8aa3b, v122\n"
   "v_pk_fma_f32 v[124:125], v[124:125], s[62:63], v[122:123] op_sel_hi:[1,1,0]\n"
   "v_pk_fma_f32 v[126:127], v[126:127], s[62:63], v[122:123] op_sel_hi:[1,1,0]\n"
   "v_pk_fma_f32 v[128:129], v[128:129], s[62:63], v[122:123] op_sel_hi:[1,1,0]\n"
   "v_pk_fma_f32 v[130:131], v[130:131], s[62:63], v[122:123] op_sel_hi:[1,1,0]\n"
   "v_pk_fma_f32 v[132:133], v[132:133], s[62:63], v[122:123] op_sel_hi:[1,1,0]\n"
   "v_pk_fma_f32 v[134:135], v[134:135], s[62:63], v[122:123] op_sel_hi:[1,1,0]\n"
   "v_pk_fma_f32 v[136:137], v[136:137], s[62:63], v[122:123] op_sel_hi:[1,1,0]\n"
   "v_pk_fma_f32 v[138:139], v[138:139], s[62:63], v[122:123] op_sel_hi:[1,1,0]\n"
   "v_exp_f32 v124, v124\n"
   "v_exp_f32 v125, v125\n"
   "v_exp_f32 v126, v126\n"
   "v_exp_f32 v127, v127\n"
   "v_exp_f32 v128, v128\n"
   "v_exp_f32 v129, v129\n"
   "v_exp_f32 v130, v130\n"
   "v_exp_f32 v131, v131\n"
   "v_exp_f32_e64 v132, v132\n"
   "s_add_u32 m0, s80, 0x16000\n"
   "s_add_u32 s58, s83, 0x81000\n"
   "buffer_load_dwordx4 v189, s[24:27], s58 offen lds\n"
   "buffer_load_dwordx4 v190, s[24:27], s58 offen offset:1024 lds\n"
   "buffer_load_dwordx4 v191, s[24:27], s58 offen offset:2048 lds\n"
   "v_exp_f32 v133, v133\n"
   "v_exp_f32 v134, v134\n"
   "v_exp_f32 v135, v135\n"
   "v_exp_f32 v136, v136\n"
   "v_exp_f32 v137, v137\n"
   "v_exp_f32 v138, v138\n"
   "v_exp_f32_e64 v139, v139\n"
   "v_pk_add_f32 v[122:123], v[124:125], v[126:127]\n"
   "v_pk_add_f32 v[122:123], v[122:123], v[128:129]\n"
   "v_pk_add_f32 v[122:123], v[122:123], v[130:131]\n"
   "v_pk_add_f32 v[122:123], v[122:123], v[132:133]\n"
   "v_pk_add_f32 v[122:123], v[122:123], v[134:135]\n"
   "v_pk_add_f32 v[122:123], v[122:123], v[136:137]\n"
   "v_pk_add_f32 v[122:123], v[122:123], v[138:139]\n"
   "v_add_f32_e64 v122, v122, v123\n"
   "v_rcp_f32 v122, v122\n"
   "s_nop 0\n"
   "v_pk_mul_f32 v[124:125], v[124:125], v[122:123] op_sel_hi:[1,0]\n"
   "v_pk_mul_f32 v[126:127], v[126:127], v[122:123] op_sel_hi:[1,0]\n"
   "v_pk_mul_f32 v[128:129], v[128:129], v[122:123] op_sel_hi:[1,0]\n"
   "s_add_u32 m0, s80, 0x19000\n"
   "s_add_u32 s58, s83, 0x96800\n"
   "buffer_load_dwordx4 v189, s[24:27], s58 offen lds\n"
   "buffer_load_dwordx4 v190, s[24:27], s58 offen offset:1024 lds\n"
   "buffer_load_dwordx4 v191, s[24:27], s58 offen offset:2048 lds\n"
   "v_pk_mul_f32 v[130:131], v[130:131], v[122:123] op_sel_hi:[1,0]\n"
   "v_pk_mul_f32 v[132:133], v[132:133], v[122:123] op_sel_hi:[1,0]\n"
   "v_pk_mul_f32 v[134:135], v[134:135], v[122:123] op_sel_hi:[1,0]\n"
   "v_pk_mul_f32 v[136:137], v[136:137], v[122:123] op_sel_hi:[1,0]\n"
   "v_pk_mul_f32 v[138:139], v[138:139], v[122:123] op_sel_hi:[1,0]\n"
   "v_cvt_pk_bf16_f32 v124, v124, v125\n"
   "v_cvt_pk_bf16_f32 v125, v126, v127\n"
   "v_cvt_pk_bf16_f32 v126, v128, v129\n"
   "v_cvt_pk_bf16_f32 v127, v130, v131\n"
   "v_cvt_pk_bf16_f32 v128, v132, v133\n"
   "v_cvt_pk_bf16_f32 v129, v134, v135\n"
   "v_cvt_pk_bf16_f32 v130, v136, v137\n"
   "v_cvt_pk_bf16_f32 v131, v138, v139\n"
   "ds_write_b128 v175, v[124:127]\n"
   "ds_write_b128 v184, v[128:131]\n"
   "ds_read_b64_tr_b16 v[16:17], v186\n"
   "ds_read_b64_tr_b16 v[18:19], v186 offset:1024\n"
   "ds_read_b64_tr_b16 v[20:21], v176\n"
   "ds_read_b64_tr_b16 v[22:23], v176 offset:1024\n"
   "s_add_u32 m0, s61, 0\n"
   "s_add_u32 s58, s66, 0\n"
   "buffer_load_dwordx4 v11, s[20:23], s58 offen lds\n"
   "buffer_load_dwordx4 v12, s[20:23], s58 offen offset:1024 lds\n"
   "buffer_load_dwordx4 v13, s[20:23], s58 offen offset:2048 lds\n"
   "buffer_load_dwordx4 v174, s[20:23], s58 offen offset:3072 lds\n"
   "v_max3_f32 v122, v140, v141, v142\n"
   "v_max3_f32 v122, v122, v143, v144\n"
   "v_max3_f32 v122, v122, v145, v146\n"
   "v_max3_f32 v122, v122, v147, v148\n"
   "v_max3_f32 v122, v122, v149, v150\n"
   "v_max3_f32 v122, v122, v151, v152\n"
   "v_max3_f32 v122, v122, v153, v154\n"
   "v_max_f32_e64 v122, v122, v155\n"
   "v_mul_f32 v122, 0xbfb8aa3b, v122\n"
   "v_pk_fma_f32 v[140:141], v[140:141], s[62:63], v[122:123] op_sel_hi:[1,1,0]\n"
   "v_pk_fma_f32 v[142:143], v[142:143], s[62:63], v[122:123] op_sel_hi:[1,1,0]\n"
   "v_lshlrev_b32_e64 v120, 7, v165\n"
   "s_and_b32 s59, s2, 7\n"
   "s_lshl_b32 s59, s59, 10\n"
   "s_add_u32 s59, s59, 0x8000\n"
   "buffer_load_dwordx2 v[120:121], v120, s[44:47], s59 offen sc1\n"
   "v_pk_fma_f32 v[144:145], v[144:145], s[62:63], v[122:123] op_sel_hi:[1,1,0]\n"
   "v_pk_fma_f32 v[146:147], v[146:147], s[62:63], v[122:123] op_sel_hi:[1,1,0]\n"
   "v_pk_fma_f32 v[148:149], v[148:149], s[62:63], v[122:123] op_sel_hi:[1,1,0]\n"
   "s_add_u32 m0, s61, 0x4000\n"
   "s_add_u32 s58, s66, 0x20000\n"
   "buffer_load_dwordx4 v11, s[20:23], s58 offen lds\n"
   "buffer_load_dwordx4 v12, s[20:23], s58 offen offset:1024 lds\n"
   "buffer_load_dwordx4 v13, s[20:23], s58 offen offset:2048 lds\n"
   "buffer_load_dwordx4 v174, s[20:23], s58 offen offset:3072 lds\n"
   "v_pk_fma_f32 v[150:151], v[150:151], s[62:63], v[122:123] op_sel_hi:[1,1,0]\n"
   "v_pk_fma_f32 v[152:153], v[152:153], s[62:63], v[122:123] op_sel_hi:[1,1,0]\n"
   "v_pk_fma_f32 v[154:155], v[154:155], s[62:63], v[122:123] op_sel_hi:[1,1,0]\n"
   "v_exp_f32 v140, v140\n"
   "v_exp_f32 v141, v141\n"
   "v_exp_f32 v142, v142\n"
   "v_exp_f32 v143, v143\n"
   "v_exp_f32 v144, v144\n"
   "v_exp_f32 v145, v145\n"
   "v_exp_f32 v146, v146\n"
   "v_exp_f32 v147, v147\n"
   "v_exp_f32 v148, v148\n"
   "v_exp_f32 v149, v149\n"
   "v_exp_f32 v150, v150\n"
   "v_exp_f32 v151, v151\n"
   "v_exp_f32 v152, v152\n"
   "v_exp_f32 v153, v153\n"
   "v_exp_f32 v154, v154\n"
   "v_exp_f32 v155, v155\n"
   "v_pk_add_f32 v[122:123], v[140:141], v[142:143]\n"
   "v_pk_add_f32 v[122:123], v[122:123], v[144:145]\n"
   "v_pk_add_f32 v[122:123], v[122:123], v[146:147]\n"
   "v_pk_add_f32 v[122:123], v[122:123], v[148:149]\n"
   "v_pk_add_f32 v[122:123], v[122:123], v[150:151]\n"
   "v_pk_add_f32 v[122:123], v[122:123], v[152:153]\n"
   "v_pk_add_f32 v[122:123], v[122:123], v[154:155]\n"
   "v_add_f32_e64 v122, v122, v123\n"
   "v_rcp_f32 v122, v122\n"
   "s_nop 0\n"
   "v_pk_mul_f32 v[140:141], v[140:141], v[122:123] op_sel_hi:[1,0]\n"
   "v_pk_mul_f32 v[142:143], v[142:143], v[122:123] op_sel_hi:[1,0]\n"
   "v_pk_mul_f32 v[144:145], v[144:145], v[122:123] op_sel_hi:[1,0]\n"
   "v_pk_mul_f32 v[146:147], v[146:147], v[122:123] op_sel_hi:[1,0]\n"
   "v_pk_mul_f32 v[148:149], v[148:149], v[122:123] op_sel_hi:[1,0]\n"
   "v_pk_mul_f32 v[150:151], v[150:151], v[122:123] op_sel_hi:[1,0]\n"
   "v_pk_mul_f32 v[152:153], v[152:153], v[122:123] op_sel_hi:[1,0]\n"
   "v_pk_mul_f32 v[154:155], v[154:155], v[122:123] op_sel_hi:[1,0]\n"
   "v_cvt_pk_bf16_f32 v140, v140, v141\n"
   "v_cvt_pk_bf16_f32 v141, v142, v143\n"
   "v_cvt_pk_bf16_f32 v142, v144, v145\n"
   "v_cvt_pk_bf16_f32 v143, v146, v147\n"
   "v_cvt_pk_bf16_f32 v144, v148, v149\n"
   "v_cvt_pk_bf16_f32 v145, v150, v151\n"
   "v_cvt_pk_bf16_f32 v146, v152, v153\n"
   "v_cvt_pk_bf16_f32 v147, v154, v155\n"
   "ds_write_b128 v175, v[140:143] offset:2048\n"
   "ds_write_b128 v184, v[144:147] offset:2048\n"
   "ds_read_b64_tr_b16 v[24:25], v186 offset:2048\n"
   "ds_read_b64_tr_b16 v[26:27], v186 offset:3072\n"
   "ds_read_b64_tr_b16 v[28:29], v176 offset:2048\n"
   "ds_read_b64_tr_b16 v[30:31], v176 offset:3072\n"
   "s_waitcnt lgkmcnt(8)\n"
   "v_mfma_f32_16x16x32_bf16 v[192:195], v[16:19], v[80:83], 0\n"
   "s_waitcnt lgkmcnt(6)\n"
   "v_cvt_pk_bf16_f32 v200, v64, v65\n"
   "v_cvt_pk_bf16_f32 v201, v66, v67\n"
   "v_mfma_f32_16x16x32_bf16 v[196:199], v[20:23], v[80:83], 0\n"
   "v_cvt_pk_bf16_f32 v202, v68, v69\n"
   "v_mfma_f32_16x16x32_bf16 v[216:219], v[16:19], a[208:211], 0\n"
   "v_cvt_pk_bf16_f32 v203, v70, v71\n"
   "v_mfma_f32_16x16x32_bf16 v[220:223], v[20:23], a[208:211], 0\n"
   "s_waitcnt lgkmcnt(2)\n"
   "v_mfma_f32_16x16x32_bf16 v[192:195], v[24:27], v[84:87], v[192:195]\n"
   "s_waitcnt lgkmcnt(0)\n"
   "v_mfma_f32_16x16x32_bf16 v[196:199], v[28:31], v[84:87], v[196:199]\n"
   "v_mfma_f32_16x16x32_bf16 v[216:219], v[24:27], a[208:211], v[216:219]\n"
   "v_cvt_pk_bf16_f32 v204, v72, v73\n"
   "v_cvt_pk_bf16_f32 v205, v74, v75\n"
   "v_mfma_f32_16x16x32_bf16 v[220:223], v[28:31], a[208:211], v[220:223]\n"
   "v_cvt_pk_bf16_f32 v206, v76, v77\n"
   "v_cvt_pk_bf16_f32 v207, v78, v79\n"
   "v_bfi_b32 v192, v15, s86, v192\n"
   "v_bfi_b32 v194, v15, s86, v194\n"
   "buffer_store_dwordx4 v[192:195], v177, s[44:47], s88 offen sc1\n"
   "v_bfi_b32 v196, v15, s86, v196\n"
   "v_bfi_b32 v198, v15, s86, v198\n"
   "buffer_store_dwordx4 v[196:199], v177, s[44:47], s89 offen sc1\n"
   "v_cndmask_b32 v216, v216, v220, s[92:93]\n"
   "v_cndmask_b32 v217, v217, v221, s[92:93]\n"
   "v_cndmask_b32 v218, v218, v222, s[92:93]\n"
   "v_cndmask_b32 v219, v219, v223, s[92:93]\n"
   "v_bfi_b32 v216, v15, s86, v216\n"
   "v_bfi_b32 v218, v15, s86, v218\n"
   "buffer_store_dwordx4 v[216:219], v179, s[44:47], s90 offen sc1\n"
   "v_accvgpr_read_b32 v184, a228\n"
   "s_add_u32 s56, s91, 0x1f000\n"
   "buffer_load_dwordx4 v[80:83], v184, s[44:47], s56 offen sc1\n"
   "s_add_u32 m0, s61, 0x8000\n"
   "s_add_u32 s58, s66, 0x40000\n"
   "buffer_load_dwordx4 v11, s[20:23], s58 offen lds\n"
   "buffer_load_dwordx4 v12, s[20:23], s58 offen offset:1024 lds\n"
   "buffer_load_dwordx4 v13, s[20:23], s58 offen offset:2048 lds\n"
   "buffer_load_dwordx4 v174, s[20:23], s58 offen offset:3072 lds\n"
   "v_mfma_f32_16x16x32_bf16 v[124:127], a[192:195], v[200:203], 0\n"
   "v_lshl_or_b32 v226, v182, 3, v183\n"
   "v_mov_b32_e64 v225, s48\n"
   "v_lshl_or_b32 v161, v182, 14, v183\n"
   "v_mfma_f32_16x16x32_bf16 v[128:131], a[196:199], v[200:203], 0\n"
   "s_lshl_b32 s56, s1, 9\n"
   "s_add_u32 s56, s56, s70\n"
   "v_mfma_f32_16x16x32_bf16 v[132:135], a[200:203], v[200:203], 0\n"
   "s_add_u32 s67, s56, 0x100000\n"
   "s_add_u32 s73, s67, 0x8000\n"
   "s_lshr_b32 s74, s51, 1\n"
   "v_mfma_f32_16x16x32_bf16 v[136:139], a[204:207], v[200:203], 0\n"
   "s_mul_i32 s74, s74, 0x1700\n"
   "s_lshl1_add_u32 s74, s79, s74\n"
   "v_mfma_f32_16x16x32_bf16 v[140:143], a[192:195], v[204:207], 0\n"
   "s_add_u32 s74, s74, 0x4000\n"
   "s_lshr_b32 s56, s81, 2\n"
   "v_mfma_f32_16x16x32_bf16 v[144:147], a[196:199], v[204:207], 0\n"
   "s_add_u32 s74, s74, s56\n"
   "s_add_u32 s81, s81, 0x4800\n"
   "v_mov_b32_e64 v208, s48\n"
   "v_mfma_f32_16x16x32_bf16 v[148:151], a[200:203], v[204:207], 0\n"
   "v_mov_b32 v209, s49\n"
   "v_mov_b32 v210, s48\n"
   "v_mfma_f32_16x16x32_bf16 v[152:155], a[204:207], v[204:207], 0\n"
   "v_mov_b32 v211, s49\n"
   "v_cmp_gt_u32 vcc, 8, v6\n"
   "v_lshlrev_b32_e64 v1, 7, v6\n"
   "s_add_u32 s56, s72, 0xe0000\n"
   "s_cmp_eq_u32 s51, 1\n"
   "s_cselect_b32 s75, s56, 0x40000000\n"
   "v_cndmask_b32 v188, v14, v9, vcc\n"
   "v_cndmask_b32 v112, v14, v1, vcc\n"
   "s_lshr_b32 s56, s70, 6\n"
   "s_add_u32 s56, s56, 0xe0000\n"
   "s_cmp_eq_u32 s51, 0\n"
   "s_cselect_b32 s76, s56, 0x40000000\n"
   "s_cselect_b32 s88, 0xff, 0\n"
   "s_mov_b32 s89, 0\n"
   "v_accvgpr_read_b32 v113, a233\n"
   "v_accvgpr_read_b32 v177, a226\n"
   "v_accvgpr_read_b32 v179, a227\n"
   "v_accvgpr_read_b32 v172, a232\n"
   "v_xor_b32 v173, 32, v172\n"
   "v_xor_b32 v175, 64, v172\n"
   "v_xor_b32 v176, 0x60, v172\n"
   "v_max3_f32 v122, v124, v125, v126\n"
   "v_max3_f32 v122, v122, v127, v128\n"
   "v_max3_f32 v122, v122, v129, v130\n"
   "v_max3_f32 v122, v122, v131, v132\n"
   "v_max3_f32 v122, v122, v133, v134\n"
   "v_max3_f32 v122, v122, v135, v136\n"
   "v_max3_f32 v122, v122, v137, v138\n"
   "v_max_f32_e64 v122, v122, v139\n"
   "v_mul_f32 v122, 0xbfb8aa3b, v122\n"
   "v_pk_fma_f32 v[124:125], v[124:125], s[62:63], v[122:123] op_sel_hi:[1,1,0]\n"
   "v_pk_fma_f32 v[126:127], v[126:127], s[62:63], v[122:123] op_sel_hi:[1,1,0]\n"
   "s_waitcnt vmcnt(12)\n"
   "v_subrev_u32 v120, s68, v120\n"
   "v_add_u32 v3, 0x64, v120\n"
   "v_cmp_gt_u32 vcc, 0xc8, v3\n"
   "v_min_u32 v121, v15, v121\n"
   "v_mov_b32 v4, 0x8c02bc\n"
   "v_lshl_or_b32 v120, v3, 16, v121\n"
   "v_cndmask_b32 v120, v4, v120, vcc\n"
   "s_nop 1\n"
   "v_max_u32_dpp v120, v120, v120 row_ror:8 row_mask:0xf bank_mask:0xf\n"
   "s_nop 0\n"
   "v_readlane_b32 s60, v120, 0\n"
   "s_nop 0\n"
   "s_max_u32 s53, s60, s60\n"
   "v_readlane_b32 s60, v120, 16\n"
   "s_nop 0\n"
   "s_max_u32 s53, s53, s60\n"
   "v_readlane_b32 s60, v120, 32\n"
   "s_nop 0\n"
   "s_max_u32 s53, s53, s60\n"
   "v_readlane_b32 s60, v120, 48\n"
   "s_nop 0\n"
   "s_max_u32 s53, s53, s60\n"
   "s_and_b32 s0, s53, 0xffff\n"
   "s_lshr_b32 s60, s53, 16\n"
   "s_add_u32 s68, s68, s60\n"
   "s_sub_u32 s68, s68, 0x64\n"
   "s_mul_i32 s69, s0, 0xfb\n"
   "s_lshr_b32 s69, s69, 10\n"
   ".Lfb_gate16:\n"
   "s_memrealtime s[52:53]\n"
   "s_waitcnt lgkmcnt(0)\n"
   "s_sub_u32 s60, s52, s68\n"
   "s_cmp_lt_u32 s60, s69\n"
   "s_cbranch_scc1 .Lfb_gate16\n"
   "buffer_load_dwordx4 v[16:19], v9, s[44:47], s71 offen sc1\n"
   "buffer_load_dwordx4 v[20:23], v9, s[44:47], s71 offen offset:1024 sc1\n"
   "buffer_load_dwordx4 v[24:27], v9, s[44:47], s71 offen offset:2048 sc1\n"
   "buffer_load_dwordx4 v[28:31], v9, s[44:47], s71 offen offset:3072 sc1\n"
   "s_waitcnt lgkmcnt(0)\n"
   "s_barrier\n"
   "s_add_u32 m0, s61, 0xc000\n"
   "s_add_u32 s58, s66, 0x60000\n"
   "buffer_load_dwordx4 v11, s[20:23], s58 offen lds\n"
   "buffer_load_dwordx4 v12, s[20:23], s58 offen offset:1024 lds\n"
   "buffer_load_dwordx4 v13, s[20:23], s58 offen offset:2048 lds\n"
   "buffer_load_dwordx4 v174, s[20:23], s58 offen offset:3072 lds\n"
   "ds_read_b64_tr_b16 a[24:25], v117\n"
   "ds_read_b64_tr_b16 a[26:27], v117 offset:6144\n"
   "ds_read_b64_tr_b16 a[28:29], v118\n"
   "ds_read_b64_tr_b16 a[30:31], v118 offset:6144\n"
   "ds_read_b64_tr_b16 a[32:33], v119\n"
   "ds_read_b64_tr_b16 a[34:35], v119 offset:6144\n"
   "ds_read_b64_tr_b16 a[36:37], v117 offset:12288\n"
   "ds_read_b64_tr_b16 a[38:39], v117 offset:18432\n"
   "ds_read_b64_tr_b16 a[40:41], v118 offset:12288\n"
   "ds_read_b64_tr_b16 a[42:43], v118 offset:18432\n"
   "ds_read_b64_tr_b16 a[44:45], v119 offset:12288\n"
   "ds_read_b64_tr_b16 a[46:47], v119 offset:18432\n"
   "ds_read_b64_tr_b16 a[48:49], v117 offset:24576\n"
   "ds_read_b64_tr_b16 a[50:51], v117 offset:30720\n"
   "ds_read_b64_tr_b16 a[52:53], v118 offset:24576\n"
   "ds_read_b64_tr_b16 a[54:55], v118 offset:30720\n"
   "ds_read_b64_tr_b16 a[56:57], v119 offset:24576\n"
   "ds_read_b64_tr_b16 a[58:59], v119 offset:30720\n"
   "ds_read_b64_tr_b16 a[60:61], v117 offset:36864\n"
   "ds_read_b64_tr_b16 a[62:63], v117 offset:43008\n"
   "ds_read_b64_tr_b16 a[64:65], v118 offset:36864\n"
   "ds_read_b64_tr_b16 a[66:67], v118 offset:43008\n"
   "ds_read_b64_tr_b16 a[68:69], v119 offset:36864\n"
   "ds_read_b64_tr_b16 a[70:71], v119 offset:43008\n"
   "ds_read_b64_tr_b16 a[72:73], v114 offset:24576\n"
   "ds_read_b64_tr_b16 a[74:75], v114 offset:30720\n"
   "ds_read_b64_tr_b16 a[76:77], v115 offset:24576\n"
   "ds_read_b64_tr_b16 a[78:79], v115 offset:30720\n"
   "ds_read_b64_tr_b16 a[80:81], v116 offset:24576\n"
   "ds_read_b64_tr_b16 a[82:83], v116 offset:30720\n"
   "ds_read_b64_tr_b16 a[84:85], v114 offset:36864\n"
   "ds_read_b64_tr_b16 a[86:87], v114 offset:43008\n"
   "ds_read_b64_tr_b16 a[88:89], v115 offset:36864\n"
   "ds_read_b64_tr_b16 a[90:91], v115 offset:43008\n"
   "ds_read_b64_tr_b16 a[92:93], v116 offset:36864\n"
   "ds_read_b64_tr_b16 a[94:95], v116 offset:43008\n"
   "s_cmp_lt_u32 s91, 0x40000000\n"
   "s_cbranch_scc0 .Lfb_skip19\n"
   "s_waitcnt vmcnt(12)\n"
   "v_xor_b32 v80, s86, v80\n"
   "v_xor_b32 v82, s86, v82\n"
   "v_or3_b32 v10, v80, v82, v82\n"
   "v_and_b32 v10, v15, v10\n"
   "v_cmp_ne_u32 vcc, 0, v10\n"
   "v_permlane32_swap_b32 v80, v82\n"
   "v_permlane32_swap_b32 v81, v83\n"
   "v_add_f32 v80, v80, v82\n"
   "v_add_f32 v81, v81, v83\n"
   "v_mov_b32 v204, v80\n"
   "v_mov_b32 v205, v81\n"
   "s_nop 0\n"
   "v_permlane16_swap_b32 v80, v204\n"
   "v_permlane16_swap_b32 v81, v205\n"
   "v_add_f32 v80, v80, v204\n"
   "v_add_f32 v81, v81, v205\n"
   "s_nop 0\n"
   "v_add_f32_dpp v80, v80, v80 row_ror:8 row_mask:0xf bank_mask:0xf\n"
   "v_add_f32_dpp v81, v81, v81 row_ror:8 row_mask:0xf bank_mask:0xf\n"
   "v_cvt_pk_bf16_f32 v224, v80, v81\n"
   "s_cbranch_vccnz .Lfb_stub17\n"
   ".Lfb_back18:\n"
   "s_add_u32 s60, s91, 0x2000\n"
   "s_mov_b32 exec_lo, 0xff\n"
   "s_mov_b32 exec_hi, 0xff\n"
   "buffer_store_dwordx2 v[224:225], v226, s[44:47], s60 offen sc1\n"
   "s_mov_b64 exec, -1\n"
   ".Lfb_skip19:\n"
   "s_nop 0\n"
   "v_pk_fma_f32 v[128:129], v[128:129], s[62:63], v[122:123] op_sel_hi:[1,1,0]\n"
   "v_pk_fma_f32 v[130:131], v[130:131], s[62:63], v[122:123] op_sel_hi:[1,1,0]\n"
   "v_pk_fma_f32 v[132:133], v[132:133], s[62:63], v[122:123] op_sel_hi:[1,1,0]\n"
   "v_pk_fma_f32 v[134:135], v[134:135], s[62:63], v[122:123] op_sel_hi:[1,1,0]\n"
   "v_pk_fma_f32 v[136:137], v[136:137], s[62:63], v[122:123] op_sel_hi:[1,1,0]\n"
   "v_pk_fma_f32 v[138:139], v[138:139], s[62:63], v[122:123] op_sel_hi:[1,1,0]\n"
   "v_exp_f32 v124, v124\n"
   "v_exp_f32 v125, v125\n"
   "v_exp_f32 v126, v126\n"
   "v_exp_f32 v127, v127\n"
   "v_exp_f32_e64 v128, v128\n"
   "v_exp_f32 v129, v129\n"
   "s_waitcnt lgkmcnt(0)\n"
   "s_barrier\n"
   "s_lshr_b32 s58, s61, 1\n"
   "s_add_u32 m0, s58, 0x14000\n"
   "s_lshl2_add_u32 s3, s61, s70\n"
   "s_add_u32 s58, s3, s79\n"
   "buffer_load_dwordx4 v11, s[8:11], s58 offen lds\n"
   "buffer_load_dwordx4 v12, s[8:11], s58 offen offset:1024 lds\n"
   "s_add_u32 m0, s80, 0x11000\n"
   "s_add_u32 s58, s83, 0xac000\n"
   "buffer_load_dwordx4 v189, s[24:27], s58 offen lds\n"
   "buffer_load_dwordx4 v190, s[24:27], s58 offen offset:1024 lds\n"
   "buffer_load_dwordx4 v191, s[24:27], s58 offen offset:2048 lds\n"
   "v_exp_f32 v130, v130\n"
   "v_exp_f32 v131, v131\n"
   "v_exp_f32 v132, v132\n"
   "v_exp_f32 v133, v133\n"
   "v_exp_f32 v134, v134\n"
   "v_exp_f32 v135, v135\n"
   "v_exp_f32 v136, v136\n"
   "v_exp_f32 v137, v137\n"
   "v_exp_f32 v138, v138\n"
   "v_exp_f32 v139, v139\n"
   "v_pk_add_f32 v[122:123], v[124:125], v[126:127]\n"
   "s_waitcnt vmcnt(12)\n"
   "v_xor_b32 v16, s86, v16\n"
   "v_xor_b32 v18, s86, v18\n"
   "s_waitcnt vmcnt(11)\n"
   "v_xor_b32 v20, s86, v20\n"
   "v_xor_b32 v22, s86, v22\n"
   "v_pk_add_f32 v[200:201], v[16:17], v[20:21]\n"
   "v_pk_add_f32 v[202:203], v[18:19], v[22:23]\n"
   "s_waitcnt vmcnt(10)\n"
   "v_xor_b32 v24, s86, v24\n"
   "v_xor_b32_e64 v26, s86, v26\n"
   "v_pk_add_f32 v[200:201], v[200:201], v[24:25]\n"
   "v_pk_add_f32 v[202:203], v[202:203], v[26:27]\n"
   "s_waitcnt vmcnt(9)\n"
   "v_xor_b32 v28, s86, v28\n"
   "v_xor_b32_e64 v30, s86, v30\n"
   "v_pk_add_f32 v[200:201], v[200:201], v[28:29]\n"
   "v_pk_add_f32 v[202:203], v[202:203], v[30:31]\n"
   "v_or3_b32 v10, v16, v18, v20\n"
   "v_or3_b32 v10, v10, v22, v24\n"
   "v_permlane32_swap_b32 v200, v202\n"
   "v_permlane32_swap_b32 v201, v203\n"
   "v_add_f32 v200, v200, v202\n"
   "v_add_f32 v201, v201, v203\n"
   "v_mov_b32 v204, v200\n"
   "v_mov_b32 v205, v201\n"
   "v_or3_b32 v10, v10, v26, v28\n"
   "v_or3_b32 v10, v10, v30, v30\n"
   "v_permlane16_swap_b32 v200, v204\n"
   "v_permlane16_swap_b32 v201, v205\n"
   "v_add_f32 v200, v200, v204\n"
   "v_add_f32 v201, v201, v205\n"
   "v_and_b32 v10, v15, v10\n"
   "v_cmp_ne_u32 vcc, 0, v10\n"
   "v_add_f32_dpp v200, v200, v200 row_ror:8 row_mask:0xf bank_mask:0xf\n"
   "v_add_f32_dpp v201, v201, v201 row_ror:8 row_mask:0xf bank_mask:0xf\n"
   "v_cvt_pk_bf16_f32 v224, v200, v201\n"
   "s_cbranch_vccnz .Lfb_stub14\n"
   ".Lfb_back15:\n"
   "s_mov_b32 exec_lo, 0xff\n"
   "s_mov_b32 exec_hi, 0xff\n"
   "buffer_store_dwordx2 v[224:225], v226, s[44:47], s82 offen sc1\n"
   "s_mov_b64 exec, -1\n"
   "s_nop 0\n"
   "s_add_u32 m0, s80, 0x16000\n"
   "s_add_u32 s58, s83, 0xc1800\n"
   "buffer_load_dwordx4 v189, s[24:27], s58 offen lds\n"
   "buffer_load_dwordx4 v190, s[24:27], s58 offen offset:1024 lds\n"
   "buffer_load_dwordx4 v191, s[24:27], s58 offen offset:2048 lds\n"
   "s_add_u32 m0, s80, 0x19000\n"
   "s_add_u32 s58, s83, 0xd7000\n"
   "buffer_load_dwordx4 v189, s[24:27], s58 offen lds\n"
   "buffer_load_dwordx4 v190, s[24:27], s58 offen offset:1024 lds\n"
   "buffer_load_dwordx4 v191, s[24:27], s58 offen offset:2048 lds\n"
   "v_pk_add_f32 v[122:123], v[122:123], v[128:129]\n"
   "v_pk_add_f32 v[122:123], v[122:123], v[130:131]\n"
   "v_pk_add_f32 v[122:123], v[122:123], v[132:133]\n"
   "v_pk_add_f32 v[122:123], v[122:123], v[134:135]\n"
   "v_pk_add_f32 v[122:123], v[122:123], v[136:137]\n"
   "v_pk_add_f32 v[122:123], v[122:123], v[138:139]\n"
   "v_add_f32_e64 v122, v122, v123\n"
   "v_rcp_f32 v122, v122\n"
   "s_nop 0\n"
   "v_pk_mul_f32 v[124:125], v[124:125], v[122:123] op_sel_hi:[1,0]\n"
   "v_pk_mul_f32 v[126:127], v[126:127], v[122:123] op_sel_hi:[1,0]\n"
   "v_pk_mul_f32 v[128:129], v[128:129], v[122:123] op_sel_hi:[1,0]\n"
   "v_pk_mul_f32 v[130:131], v[130:131], v[122:123] op_sel_hi:[1,0]\n"
   "v_pk_mul_f32 v[132:133], v[132:133], v[122:123] op_sel_hi:[1,0]\n"
   "v_pk_mul_f32 v[134:135], v[134:135], v[122:123] op_sel_hi:[1,0]\n"
   "v_pk_mul_f32 v[136:137], v[136:137], v[122:123] op_sel_hi:[1,0]\n"
   "v_pk_mul_f32 v[138:139], v[138:139], v[122:123] op_sel_hi:[1,0]\n"
   "v_cvt_pk_bf16_f32 v124, v124, v125\n"
   "v_cvt_pk_bf16_f32 v125, v126, v127\n"
   "v_cvt_pk_bf16_f32 v126, v128, v129\n"
   "v_cvt_pk_bf16_f32 v127, v130, v131\n"
   "v_cvt_pk_bf16_f32 v128, v132, v133\n"
   "v_cvt_pk_bf16_f32 v129, v134, v135\n"
   "v_cvt_pk_bf16_f32 v130, v136, v137\n"
   "v_cvt_pk_bf16_f32 v131, v138, v139\n"
   "v_permlane32_swap_b32 v124, v128\n"
   "v_permlane32_swap_b32 v125, v129\n"
   "v_permlane32_swap_b32 v126, v130\n"
   "v_permlane32_swap_b32 v127, v131\n"
   "v_max3_f32 v122, v140, v141, v142\n"
   "v_max3_f32 v122, v122, v143, v144\n"
   "v_max3_f32 v122, v122, v145, v146\n"
   "v_max3_f32 v122, v122, v147, v148\n"
   "v_max3_f32 v122, v122, v149, v150\n"
   "v_max3_f32 v122, v122, v151, v152\n"
   "v_max3_f32 v122, v122, v153, v154\n"
   "v_max_f32_e64 v122, v122, v155\n"
   "v_mul_f32 v122, 0xbfb8aa3b, v122\n"
   "v_pk_fma_f32 v[140:141], v[140:141], s[62:63], v[122:123] op_sel_hi:[1,1,0]\n"
   "v_pk_fma_f32 v[142:143], v[142:143], s[62:63], v[122:123] op_sel_hi:[1,1,0]\n"
   "v_pk_fma_f32 v[144:145], v[144:145], s[62:63], v[122:123] op_sel_hi:[1,1,0]\n"
   "v_pk_fma_f32 v[146:147], v[146:147], s[62:63], v[122:123] op_sel_hi:[1,1,0]\n"
   "v_pk_fma_f32 v[148:149], v[148:149], s[62:63], v[122:123] op_sel_hi:[1,1,0]\n"
   "v_pk_fma_f32 v[150:151], v[150:151], s[62:63], v[122:123] op_sel_hi:[1,1,0]\n"
   "v_pk_fma_f32 v[152:153], v[152:153], s[62:63], v[122:123] op_sel_hi:[1,1,0]\n"
   "v_pk_fma_f32 v[154:155], v[154:155], s[62:63], v[122:123] op_sel_hi:[1,1,0]\n"
   "v_exp_f32 v140, v140\n"
   "v_exp_f32 v141, v141\n"
   "v_exp_f32 v142, v142\n"
   "v_exp_f32 v143, v143\n"
   "v_exp_f32 v144, v144\n"
   "v_exp_f32 v145, v145\n"
   "v_exp_f32 v146, v146\n"
   "v_exp_f32 v147, v147\n"
   "v_exp_f32 v148, v148\n"
   "v_exp_f32 v149, v149\n"
   "v_exp_f32 v150, v150\n"
   "v_exp_f32 v151, v151\n"
   "v_exp_f32 v152, v152\n"
   "v_exp_f32 v153, v153\n"
   "v_exp_f32 v154, v154\n"
   "v_exp_f32 v155, v155\n"
   "v_pk_add_f32 v[122:123], v[140:141], v[142:143]\n"
   "v_pk_add_f32 v[122:123], v[122:123], v[144:145]\n"
   "v_pk_add_f32 v[122:123], v[122:123], v[146:147]\n"
   "v_pk_add_f32 v[122:123], v[122:123], v[148:149]\n"
   "v_pk_add_f32 v[122:123], v[122:123], v[150:151]\n"
   "v_pk_add_f32 v[122:123], v[122:123], v[152:153]\n"
   "v_pk_add_f32 v[122:123], v[122:123], v[154:155]\n"
   "v_add_f32_e64 v122, v122, v123\n"
   "v_rcp_f32 v122, v122\n"
   "s_nop 0\n"
   "v_pk_mul_f32 v[140:141], v[140:141], v[122:123] op_sel_hi:[1,0]\n"
   "v_pk_mul_f32 v[142:143], v[142:143], v[122:123] op_sel_hi:[1,0]\n"
   "v_pk_mul_f32 v[144:145], v[144:145], v[122:123] op_sel_hi:[1,0]\n"
   "v_pk_mul_f32 v[146:147], v[146:147], v[122:123] op_sel_hi:[1,0]\n"
   "v_pk_mul_f32 v[148:149], v[148:149], v[122:123] op_sel_hi:[1,0]\n"
   "v_pk_mul_f32 v[150:151], v[150:151], v[122:123] op_sel_hi:[1,0]\n"
   "v_pk_mul_f32 v[152:153], v[152:153], v[122:123] op_sel_hi:[1,0]\n"
   "v_pk_mul_f32 v[154:155], v[154:155], v[122:123] op_sel_hi:[1,0]\n"
   "v_cvt_pk_bf16_f32 v140, v140, v141\n"
   "v_cvt_pk_bf16_f32 v141, v142, v143\n"
   "v_cvt_pk_bf16_f32 v142, v144, v145\n"
   "v_cvt_pk_bf16_f32 v143, v146, v147\n"
   "v_cvt_pk_bf16_f32 v144, v148, v149\n"
   "v_cvt_pk_bf16_f32 v145, v150, v151\n"
   "v_cvt_pk_bf16_f32 v146, v152, v153\n"
   "v_cvt_pk_bf16_f32 v147, v154, v155\n"
   "v_permlane32_swap_b32 v140, v144\n"
   "v_permlane32_swap_b32 v141, v145\n"
   "v_permlane32_swap_b32 v142, v146\n"
   "v_permlane32_swap_b32 v143, v147\n"
   "s_waitcnt vmcnt(7)\n"
   "s_barrier\n"
   "buffer_load_dwordx4 v[224:227], v188, s[44:47], s74 offen sc1\n"
   "buffer_load_dwordx4 v[16:19], v178, s[44:47], s81 offen sc1\n"
   "s_mul_i32 s69, s0, 0x1b2\n"
   "s_lshr_b32 s69, s69, 10\n"
   ".Lfb_gate22:\n"
   "s_memrealtime s[52:53]\n"
   "s_waitcnt lgkmcnt(0)\n"
   "s_sub_u32 s60, s52, s68\n"
   "s_cmp_lt_u32 s60, s69\n"
   "s_cbranch_scc1 .Lfb_gate22\n"
   "buffer_load_dwordx4 v[96:99], v177, s[44:47], s87 offen sc1\n"
   "buffer_load_dwordx4 v[100:103], v177, s[44:47], s87 offen offset:256 sc1\n"
   "buffer_load_dwordx4 v[104:107], v179, s[44:47], s87 offen offset:2048 sc1\n"
   "buffer_load_dwordx4 v[108:111], v179, s[44:47], s87 offen offset:2064 sc1\n"
   "s_add_u32 m0, s80, 0x1c000\n"
   "s_add_u32 s58, s83, 0xec800\n"
   "buffer_load_dwordx4 v189, s[24:27], s58 offen lds\n"
   "buffer_load_dwordx4 v190, s[24:27], s58 offen offset:1024 lds\n"
   "buffer_load_dwordx4 v191, s[24:27], s58 offen offset:2048 lds\n"
   "s_add_u32 m0, s80, 0x1f000\n"
   "s_add_u32 s58, s83, 0x102000\n"
   "buffer_load_dwordx4 v189, s[24:27], s58 offen lds\n"
   "buffer_load_dwordx4 v190, s[24:27], s58 offen offset:1024 lds\n"
   "buffer_load_dwordx4 v191, s[24:27], s58 offen offset:2048 lds\n"
   "ds_read_b64_tr_b16 a[96:97], v114 offset:4096\n"
   "ds_read_b64_tr_b16 a[98:99], v114 offset:10240\n"
   "ds_read_b64_tr_b16 a[100:101], v115 offset:4096\n"
   "ds_read_b64_tr_b16 a[102:103], v115 offset:10240\n"
   "ds_read_b64_tr_b16 a[104:105], v116 offset:4096\n"
   "ds_read_b64_tr_b16 a[106:107], v116 offset:10240\n"
   "ds_read_b64_tr_b16 a[192:193], v172\n"
   "ds_read_b64_tr_b16 a[194:195], v172 offset:1024\n"
   "ds_read_b64_tr_b16 a[196:197], v173\n"
   "ds_read_b64_tr_b16 a[198:199], v173 offset:1024\n"
   "ds_read_b64_tr_b16 a[200:201], v175\n"
   "ds_read_b64_tr_b16 a[202:203], v175 offset:1024\n"
   "ds_read_b64_tr_b16 a[204:205], v176\n"
   "ds_read_b64_tr_b16 a[206:207], v176 offset:1024\n"
   "s_mov_b64 s[62:63], 0xff\n"
   "s_waitcnt vmcnt(11)\n"
   "v_cndmask_b32 v225, v208, v225, s[62:63]\n"
   "v_cndmask_b32 v227, v208, v227, s[62:63]\n"
   "v_xor_b32_e64 v10, s48, v225\n"
   "v_bitop3_b32 v10, v227, s48, v10 bitop3:0xbe\n"
   "s_waitcnt vmcnt(10)\n"
   "v_bitop3_b32 v10, v17, s48, v10 bitop3:0xbe\n"
   "v_bitop3_b32 v10, v19, s48, v10 bitop3:0xbe\n"
   "v_cmp_ne_u32 vcc, 0, v10\n"
   "s_nop 0\n"
   "s_cbranch_vccnz .Lfb_stub20\n"
   ".Lfb_back21:\n"
   "v_lshlrev_b32 v200, 16, v16\n"
   "v_and_b32 v201, s65, v16\n"
   "v_lshlrev_b32 v202, 16, v18\n"
   "v_and_b32 v203, s65, v18\n"
   "v_pk_add_f32 v[200:201], v[200:201], v[180:181] op_sel_hi:[1,0]\n"
   "v_pk_add_f32 v[202:203], v[202:203], v[180:181] op_sel_hi:[1,0]\n"
   "v_cvt_pk_bf16_f32 v204, v200, v201\n"
   "v_cvt_pk_bf16_f32 v205, v202, v203\n"
   "s_lshl_b32 s56, s51, 8\n"
   "s_add_u32 s56, s56, 0x10000\n"
   "v_lshrrev_b32 v3, 1, v178\n"
   "v_add_u32 v3, s56, v3\n"
   "s_lshl_b32 s56, s51, 6\n"
   "s_add_u32 s56, s56, 0x10800\n"
   "v_add_u32 v1, s56, v160\n"
   "v_mov_b32 v225, v226\n"
   "ds_write_b64 v3, v[204:205]\n"
   "s_mov_b64 exec, 0xff\n"
   "ds_write_b64 v1, v[224:225]\n"
   "s_mov_b64 exec, -1\n"
   "s_waitcnt vmcnt(7)\n"
   "v_mov_b32 v92, v104\n"
   "v_mov_b32 v93, v106\n"
   "s_waitcnt vmcnt(6)\n"
   "v_mov_b32 v94, v108\n"
   "v_mov_b32 v95, v110\n"
   "v_mov_b32 v88, v96\n"
   "v_mov_b32_e64 v89, v98\n"
   "v_mfma_f32_16x16x32_bf16 v[192:195], v[92:95], v[124:127], 0\n"
   "v_mov_b32 v90, v100\n"
   "v_mov_b32 v91, v102\n"
   "v_mfma_f32_16x16x32_bf16 v[196:199], v[92:95], v[128:131], 0\n"
   "v_xor_b32 v10, s48, v97\n"
   "v_bitop3_b32 v10, v99, s48, v10 bitop3:0xbe\n"
   "v_mfma_f32_16x16x32_bf16 v[216:219], v[92:95], v[140:143], 0\n"
   "v_bitop3_b32 v10, v101, s48, v10 bitop3:0xbe\n"
   "v_bitop3_b32 v10, v103, s48, v10 bitop3:0xbe\n"
   "v_mfma_f32_16x16x32_bf16 v[220:223], v[92:95], v[144:147], 0\n"
   "v_bitop3_b32 v10, v105, s48, v10 bitop3:0xbe\n"
   "v_bitop3_b32 v10, v107, s48, v10 bitop3:0xbe\n"
   "v_mfma_f32_16x16x32_bf16 v[32:35], v[88:91], v[124:127], 0\n"
   "v_bitop3_b32 v10, v109, s48, v10 bitop3:0xbe\n"
   "v_bitop3_b32 v10, v111, s48, v10 bitop3:0xbe\n"
   "v_mfma_f32_16x16x32_bf16 v[36:39], v[88:91], v[128:131], 0\n"
   "v_cmp_ne_u32 vcc, 0, v10\n"
   "v_mfma_f32_16x16x32_bf16 v[40:43], v[88:91], v[140:143], 0\n"
   "v_mfma_f32_16x16x32_bf16 v[44:47], v[88:91], v[144:147], 0\n"
   "s_cbranch_vccnz .Lfb_stub23\n"
   ".Lfb_back24:\n"
   "v_add_f32 v192, 0x358637bd, v192\n"
   "v_add_f32 v196, 0x358637bd, v196\n"
   "v_add_f32 v216, 0x358637bd, v216\n"
   "v_add_f32 v220, 0x358637bd, v220\n"
   "v_rcp_f32 v192, v192\n"
   "v_rcp_f32 v196, v196\n"
   "v_rcp_f32 v216, v216\n"
   "v_rcp_f32 v220, v220\n"
   "v_pk_mul_f32 v[32:33], v[32:33], v[192:193] op_sel_hi:[1,0]\n"
   "v_pk_mul_f32 v[34:35], v[34:35], v[192:193] op_sel_hi:[1,0]\n"
   "v_pk_mul_f32 v[36:37], v[36:37], v[196:197] op_sel_hi:[1,0]\n"
   "v_pk_mul_f32 v[38:39], v[38:39], v[196:197] op_sel_hi:[1,0]\n"
   "v_cvt_pk_bf16_f32 v80, v32, v33\n"
   "v_cvt_pk_bf16_f32 v82, v36, v37\n"
   "v_cvt_pk_bf16_f32 v81, v34, v35\n"
   "v_cvt_pk_bf16_f32 v83, v38, v39\n"
   "v_permlane32_swap_b32 v80, v82\n"
   "s_nop 0\n"
   "v_permlane32_swap_b32 v81, v83\n"
   "buffer_store_dwordx4 v[80:83], v161, s[44:47], s67 offen sc1\n"
   "v_pk_mul_f32 v[40:41], v[40:41], v[216:217] op_sel_hi:[1,0]\n"
   "v_pk_mul_f32 v[42:43], v[42:43], v[216:217] op_sel_hi:[1,0]\n"
   "v_pk_mul_f32 v[44:45], v[44:45], v[220:221] op_sel_hi:[1,0]\n"
   "v_pk_mul_f32 v[46:47], v[46:47], v[220:221] op_sel_hi:[1,0]\n"
   "v_cvt_pk_bf16_f32 v84, v40, v41\n"
   "v_cvt_pk_bf16_f32 v86, v44, v45\n"
   "v_cvt_pk_bf16_f32 v85, v42, v43\n"
   "v_cvt_pk_bf16_f32 v87, v46, v47\n"
   "v_permlane32_swap_b32 v84, v86\n"
   "s_nop 0\n"
   "v_permlane32_swap_b32 v85, v87\n"
   "buffer_store_dwordx4 v[84:87], v161, s[44:47], s73 offen sc1\n"
   "s_add_u32 m0, s80, 0x22000\n"
   "s_add_u32 s58, s83, 0x117800\n"
   "buffer_load_dwordx4 v189, s[24:27], s58 offen lds\n"
   "buffer_load_dwordx4 v190, s[24:27], s58 offen offset:1024 lds\n"
   "buffer_load_dwordx4 v191, s[24:27], s58 offen offset:2048 lds\n"
   "s_or_b32 s56, s2, s51\n"
   "s_cmp_eq_u32 s56, 0\n"
   "s_cbranch_scc0 .Lfb_skip25\n"
   "s_mov_b64 exec, 1\n"
   "buffer_store_dwordx2 v[208:209], v160, s[44:47], 0 offen sc1\n"
   "s_mov_b64 exec, -1\n"
   ".Lfb_skip25:\n"
   "v_bfe_u32 v1, v7, 1, 2\n"
   "v_lshlrev_b32_e64 v1, 5, v1\n"
   "v_lshl_or_b32 v1, v7, 7, v1\n"
   "v_lshl_or_b32 v1, v8, 3, v1\n"
   "s_lshr_b32 s56, s61, 1\n"
   "s_add_u32 s56, s56, 0x14000\n"
   "v_add_u32 v1, s56, v1\n"
   "v_xor_b32 v2, 32, v1\n"
   "v_xor_b32 v3, 64, v1\n"
   "v_xor_b32 v4, 0x60, v1\n"
   "ds_read_b64 v[50:51], v1\n"
   "ds_read_b64 v[54:55], v2\n"
   "ds_read_b64 v[58:59], v3\n"
   "ds_read_b64 v[62:63], v4\n"
   "ds_read_b64_tr_b16 a[208:209], v172 offset:4096\n"
   "ds_read_b64_tr_b16 a[210:211], v172 offset:5120\n"
   "ds_read_b64_tr_b16 a[212:213], v173 offset:4096\n"
   "ds_read_b64_tr_b16 a[214:215], v173 offset:5120\n"
   "ds_read_b64_tr_b16 a[216:217], v175 offset:4096\n"
   "ds_read_b64_tr_b16 a[218:219], v175 offset:5120\n"
   "ds_read_b64_tr_b16 a[220:221], v176 offset:4096\n"
   "ds_read_b64_tr_b16 a[222:223], v176 offset:5120\n"
   "ds_read_b64_tr_b16 a[224:225], v172 offset:8192\n"
   "ds_read_b64_tr_b16 a[226:227], v172 offset:9216\n"
   "ds_read_b64_tr_b16 a[228:229], v173 offset:8192\n"
   "ds_read_b64_tr_b16 a[230:231], v173 offset:9216\n"
   "ds_read_b64_tr_b16 a[232:233], v175 offset:8192\n"
   "ds_read_b64_tr_b16 a[234:235], v175 offset:9216\n"
   "ds_read_b64_tr_b16 a[236:237], v176 offset:8192\n"
   "ds_read_b64_tr_b16 a[238:239], v176 offset:9216\n"
   "ds_read_b64_tr_b16 a[240:241], v172 offset:12288\n"
   "ds_read_b64_tr_b16 a[242:243], v172 offset:13312\n"
   "ds_read_b64_tr_b16 a[244:245], v173 offset:12288\n"
   "ds_read_b64_tr_b16 a[246:247], v173 offset:13312\n"
   "ds_read_b64_tr_b16 a[248:249], v175 offset:12288\n"
   "ds_read_b64_tr_b16 a[250:251], v175 offset:13312\n"
   "ds_read_b64_tr_b16 a[252:253], v176 offset:12288\n"
   "ds_read_b64_tr_b16 a[254:255], v176 offset:13312\n"
   "ds_read_b64_tr_b16 v[204:205], v172 offset:16384\n"
   "ds_read_b64_tr_b16 v[206:207], v172 offset:17408\n"
   "ds_read_b64_tr_b16 v[212:213], v173 offset:16384\n"
   "ds_read_b64_tr_b16 v[214:215], v173 offset:17408\n"
   "ds_read_b64_tr_b16 v[224:225], v175 offset:16384\n"
   "ds_read_b64_tr_b16 v[226:227], v175 offset:17408\n"
   "ds_read_b64_tr_b16 v[228:229], v176 offset:16384\n"
   "ds_read_b64_tr_b16 v[230:231], v176 offset:17408\n"
   "ds_read_b64_tr_b16 v[88:89], v172 offset:20480\n"
   "ds_read_b64_tr_b16 v[90:91], v172 offset:21504\n"
   "ds_read_b64_tr_b16 v[92:93], v173 offset:20480\n"
   "ds_read_b64_tr_b16 v[94:95], v173 offset:21504\n"
   "ds_read_b64_tr_b16 v[96:97], v175 offset:20480\n"
   "ds_read_b64_tr_b16 v[98:99], v175 offset:21504\n"
   "ds_read_b64_tr_b16 v[100:101], v176 offset:20480\n"
   "ds_read_b64_tr_b16 v[102:103], v176 offset:21504\n"
   "s_waitcnt lgkmcnt(15)\n"
   "v_lshlrev_b32 v48, 16, v50\n"
   "v_and_b32 v49, s65, v50\n"
   "v_lshlrev_b32 v50, 16, v51\n"
   "v_and_b32 v51, s65, v51\n"
   "v_lshlrev_b32 v52, 16, v54\n"
   "v_and_b32 v53, s65, v54\n"
   "v_lshlrev_b32 v54, 16, v55\n"
   "v_and_b32 v55, s65, v55\n"
   "v_lshlrev_b32 v56, 16, v58\n"
   "v_and_b32 v57, s65, v58\n"
   "v_lshlrev_b32 v58, 16, v59\n"
   "v_and_b32 v59, s65, v59\n"
   "v_lshlrev_b32 v60, 16, v62\n"
   "v_and_b32 v61, s65, v62\n"
   "v_lshlrev_b32 v62, 16, v63\n"
   "v_and_b32 v63, s65, v63\n"
   "v_and_b32 v1, 1, v7\n"
   "v_lshlrev_b32_e64 v1, 10, v1\n"
   "v_lshl_or_b32 v1, v8, 3, v1\n"
   "v_add_u32 v6, 0x10000, v1\n"
   "v_mul_u32_u24 v1, 0xac00, v182\n"
   "v_add_u32_e64 v161, v1, v183\n"
   "s_mul_i32 s71, s54, 0x2b000\n"
   "s_add_u32 s71, s71, 0x500000\n"
   "s_mul_i32 s56, s51, 3\n"
   "s_add_u32 s57, s56, s84\n"
   "s_lshl_b32 s57, s57, 9\n"
   "s_add_u32 s57, s57, s71\n"
   "s_cmp_lt_u32 s56, s85\n"
   "s_cselect_b32 s82, s57, 0x40000000\n"
   "s_add_u32 s74, s82, 0x15800\n"
   "s_add_u32 s56, s56, 1\n"
   "s_add_u32 s57, s56, s84\n"
   "s_lshl_b32 s57, s57, 9\n"
   "s_add_u32 s57, s57, s71\n"
   "s_cmp_lt_u32 s56, s85\n"
   "s_cselect_b32 s87, s57, 0x40000000\n"
   "s_add_u32 s77, s87, 0x15800\n"
   "s_add_u32 s56, s56, 1\n"
   "s_add_u32 s57, s56, s84\n"
   "s_lshl_b32 s57, s57, 9\n"
   "s_add_u32 s57, s57, s71\n"
   "s_cmp_lt_u32 s56, s85\n"
   "s_cselect_b32 s73, s57, 0x40000000\n"
   "s_add_u32 s78, s73, 0x15800\n"
   "s_add_u32 s56, s56, 1\n"
   "v_lshlrev_b32 v1, 3, v8\n"
   "v_add_u32 v2, 0x10800, v1\n"
   "s_add_u32 s56, s79, 0x10400\n"
   "v_add_u32 v1, s56, v1\n"
   "s_waitcnt vmcnt(3)\n"
   "s_waitcnt lgkmcnt(0)\n"
   "s_barrier\n"
   "buffer_store_dwordx4 v[208:211], v188, s[44:47], s75 offen sc1\n"
   "s_add_u32 s75, s75, 0x8000\n"
   "s_add_u32 m0, s80, 0x25000\n"
   "s_add_u32 s58, s83, 0x12d000\n"
   "buffer_load_dwordx4 v189, s[24:27], s58 offen lds\n"
   "buffer_load_dwordx4 v190, s[24:27], s58 offen offset:1024 lds\n"
   "buffer_load_dwordx4 v191, s[24:27], s58 offen offset:2048 lds\n"
   "ds_read_b64 v[18:19], v2\n"
   "ds_read_b64 v[194:195], v1\n"
   "ds_read_b64 v[22:23], v2 offset:32\n"
   "ds_read_b64 v[198:199], v1 offset:32\n"
   "ds_read_b64 v[26:27], v2 offset:64\n"
   "ds_read_b64 v[218:219], v1 offset:64\n"
   "ds_read_b64 v[30:31], v2 offset:96\n"
   "ds_read_b64 v[222:223], v1 offset:96\n"
   "ds_read_b64 v[32:33], v6\n"
   "ds_read_b64 v[34:35], v6 offset:32\n"
   "ds_read_b64 v[36:37], v6 offset:64\n"
   "ds_read_b64 v[38:39], v6 offset:96\n"
   "ds_read_b64 v[40:41], v6 offset:128\n"
   "ds_read_b64 v[42:43], v6 offset:160\n"
   "ds_read_b64 v[44:45], v6 offset:192\n"
   "ds_read_b64 v[46:47], v6 offset:224\n"
   "ds_read_b64 v[64:65], v6 offset:256\n"
   "ds_read_b64 v[66:67], v6 offset:288\n"
   "ds_read_b64 v[68:69], v6 offset:320\n"
   "ds_read_b64 v[70:71], v6 offset:352\n"
   "ds_read_b64 v[72:73], v6 offset:384\n"
   "ds_read_b64 v[74:75], v6 offset:416\n"
   "ds_read_b64 v[76:77], v6 offset:448\n"
   "ds_read_b64 v[78:79], v6 offset:480\n"
   "ds_read_b64 v[120:121], v6 offset:512\n"
   "ds_read_b64 v[122:123], v6 offset:544\n"
   "ds_read_b64_tr_b16 v[104:105], v172 offset:24576\n"
   "ds_read_b64_tr_b16 v[106:107], v172 offset:25600\n"
   "ds_read_b64_tr_b16 v[108:109], v173 offset:24576\n"
   "ds_read_b64_tr_b16 v[110:111], v173 offset:25600\n"
   "ds_read_b64_tr_b16 v[80:81], v175 offset:24576\n"
   "ds_read_b64_tr_b16 v[82:83], v175 offset:25600\n"
   "ds_read_b64_tr_b16 v[84:85], v176 offset:24576\n"
   "ds_read_b64_tr_b16 v[86:87], v176 offset:25600\n"
   "s_mul_i32 s69, s0, 0x26d\n"
   "s_lshr_b32 s69, s69, 10\n"
   ".Lfb_gate26:\n"
   "s_memrealtime s[52:53]\n"
   "s_waitcnt lgkmcnt(0)\n"
   "s_sub_u32 s60, s52, s68\n"
   "s_cmp_lt_u32 s60, s69\n"
   "s_cbranch_scc1 .Lfb_gate26\n"
   "buffer_load_dwordx2 v[166:167], v112, s[44:47], s76 offen sc1\n"
   "v_mfma_f32_16x16x32_bf16 v[232:235], a[0:3], v[32:35], 0\n"
   "s_add_u32 m0, s80, 0x11000\n"
   "s_add_u32 s58, s83, 0x142800\n"
   "v_lshlrev_b32_e64 v16, 16, v18\n"
   "v_mfma_f32_16x16x32_bf16 v[236:239], a[4:7], v[32:35], 0\n"
   "buffer_load_dwordx4 v189, s[24:27], s58 offen lds\n"
   "v_mfma_f32_16x16x32_bf16 v[240:243], a[8:11], v[32:35], 0\n"
   "v_and_b32 v17, s65, v18\n"
   "v_lshlrev_b32 v18, 16, v19\n"
   "v_mfma_f32_16x16x32_bf16 v[232:235], a[12:15], v[36:39], v[232:235]\n"
   "v_and_b32 v19, s65, v19\n"
   "v_lshlrev_b32 v20, 16, v22\n"
   "v_mfma_f32_16x16x32_bf16 v[236:239], a[16:19], v[36:39], v[236:239]\n"
   "v_and_b32 v21, s65, v22\n"
   "v_lshlrev_b32 v22, 16, v23\n"
   "v_mfma_f32_16x16x32_bf16 v[240:243], a[20:23], v[36:39], v[240:243]\n"
   "buffer_load_dwordx4 v190, s[24:27], s58 offen offset:1024 lds\n"
   "v_mfma_f32_16x16x32_bf16 v[232:235], a[24:27], v[40:43], v[232:235]\n"
   "v_and_b32 v23, s65, v23\n"
   "v_lshlrev_b32 v24, 16, v26\n"
   "v_mfma_f32_16x16x32_bf16 v[236:239], a[28:31], v[40:43], v[236:239]\n"
   "v_and_b32 v25, s65, v26\n"
   "v_lshlrev_b32 v26, 16, v27\n"
   "v_mfma_f32_16x16x32_bf16 v[240:243], a[32:35], v[40:43], v[240:243]\n"
   "v_and_b32_e64 v27, s65, v27\n"
   "v_mfma_f32_16x16x32_bf16 v[232:235], a[36:39], v[44:47], v[232:235]\n"
   "buffer_load_dwordx4 v191, s[24:27], s58 offen offset:2048 lds\n"
   "v_mfma_f32_16x16x32_bf16 v[236:239], a[40:43], v[44:47], v[236:239]\n"
   "v_lshlrev_b32 v28, 16, v30\n"
   "v_and_b32 v29, s65, v30\n"
   "v_mfma_f32_16x16x32_bf16 v[240:243], a[44:47], v[44:47], v[240:243]\n"
   "v_lshlrev_b32 v30, 16, v31\n"
   "v_and_b32 v31, s65, v31\n"
   "v_mfma_f32_16x16x32_bf16 v[232:235], a[48:51], v[64:67], v[232:235]\n"
   "v_lshlrev_b32_e64 v192, 16, v194\n"
   "v_mfma_f32_16x16x32_bf16 v[236:239], a[52:55], v[64:67], v[236:239]\n"
   "v_and_b32_e64 v193, s65, v194\n"
   "v_mfma_f32_16x16x32_bf16 v[240:243], a[56:59], v[64:67], v[240:243]\n"
   "v_lshlrev_b32_e64 v194, 16, v195\n"
   "v_mfma_f32_16x16x32_bf16 v[232:235], a[60:63], v[68:71], v[232:235]\n"
   "v_and_b32 v195, s65, v195\n"
   "v_lshlrev_b32 v196, 16, v198\n"
   "v_mfma_f32_16x16x32_bf16 v[236:239], a[64:67], v[68:71], v[236:239]\n"
   "v_and_b32_e64 v197, s65, v198\n"
   "v_mfma_f32_16x16x32_bf16 v[240:243], a[68:71], v[68:71], v[240:243]\n"
   "v_lshlrev_b32_e64 v198, 16, v199\n"
   "v_mfma_f32_16x16x32_bf16 v[232:235], a[72:75], v[72:75], v[232:235]\n"
   "v_and_b32_e64 v199, s65, v199\n"
   "v_mfma_f32_16x16x32_bf16 v[236:239], a[76:79], v[72:75], v[236:239]\n"
   "v_lshlrev_b32 v216, 16, v218\n"
   "v_and_b32 v217, s65, v218\n"
   "v_mfma_f32_16x16x32_bf16 v[240:243], a[80:83], v[72:75], v[240:243]\n"
   "v_lshlrev_b32_e64 v218, 16, v219\n"
   "v_mfma_f32_16x16x32_bf16 v[232:235], a[84:87], v[76:79], v[232:235]\n"
   "v_and_b32_e64 v219, s65, v219\n"
   "v_mfma_f32_16x16x32_bf16 v[236:239], a[88:91], v[76:79], v[236:239]\n"
   "v_lshlrev_b32_e64 v220, 16, v222\n"
   "v_mfma_f32_16x16x32_bf16 v[240:243], a[92:95], v[76:79], v[240:243]\n"
   "v_and_b32 v221, s65, v222\n"
   "v_lshlrev_b32 v222, 16, v223\n"
   "v_mfma_f32_16x16x32_bf16 v[232:235], a[96:99], v[120:123], v[232:235]\n"
   "v_and_b32 v223, s65, v223\n"
   "v_mfma_f32_16x16x32_bf16 v[236:239], a[100:103], v[120:123], v[236:239]\n"
   "s_lshl4_add_u32 s67, s79, s3\n"
   "v_mfma_f32_16x16x32_bf16 v[240:243], a[104:107], v[120:123], v[240:243]\n"
   "s_add_u32 s67, s67, 0x300000\n"
   "s_waitcnt vmcnt(3)\n"
   "v_cmp_ne_u64 vcc, s[48:49], v[166:167]\n"
   "s_nop 0\n"
   "s_and_b64 vcc, vcc, s[88:89]\n"
   "s_cbranch_vccnz .Lfb_stub27\n"
   ".Lfb_back28:\n"
   "s_add_u32 s76, s76, 0x8000\n"
   "s_waitcnt lgkmcnt(0)\n"
   "s_barrier\n"
   "s_add_u32 s59, s3, 0x100000\n"
   "buffer_load_dwordx4 v[32:35], v9, s[44:47], s59 offen sc1\n"
   "buffer_load_dwordx4 v[36:39], v9, s[44:47], s59 offen offset:1024 sc1\n"
   "buffer_load_dwordx4 v[40:43], v9, s[44:47], s59 offen offset:2048 sc1\n"
   "buffer_load_dwordx4 v[44:47], v9, s[44:47], s59 offen offset:3072 sc1\n"
   "s_add_u32 s59, s3, 0x101000\n"
   "buffer_load_dwordx4 v[64:67], v9, s[44:47], s59 offen sc1\n"
   "s_lshl_b32 s90, s51, 14\n"
   "s_mul_i32 s91, s51, 0x58000\n"
   "s_add_u32 s91, s91, s79\n"
   "s_waitcnt vmcnt(5)\n"
   "s_barrier\n"
   "buffer_load_dwordx4 v[68:71], v9, s[44:47], s59 offen offset:1024 sc1\n"
   "ds_read_b64_tr_b16 a[108:109], v114 offset:24576\n"
   "ds_read_b64_tr_b16 a[110:111], v114 offset:30720\n"
   "ds_read_b64_tr_b16 a[112:113], v115 offset:24576\n"
   "ds_read_b64_tr_b16 a[114:115], v115 offset:30720\n"
   "ds_read_b64_tr_b16 a[116:117], v116 offset:24576\n"
   "ds_read_b64_tr_b16 a[118:119], v116 offset:30720\n"
   "buffer_load_dwordx4 v[72:75], v9, s[44:47], s59 offen offset:2048 sc1\n"
   "ds_read_b64_tr_b16 a[120:121], v114 offset:36864\n"
   "ds_read_b64_tr_b16 a[122:123], v114 offset:43008\n"
   "ds_read_b64_tr_b16 a[124:125], v115 offset:36864\n"
   "ds_read_b64_tr_b16 a[126:127], v115 offset:43008\n"
   "ds_read_b64_tr_b16 a[128:129], v116 offset:36864\n"
   "ds_read_b64_tr_b16 a[130:131], v116 offset:43008\n"
   "buffer_load_dwordx4 v[76:79], v9, s[44:47], s59 offen offset:3072 sc1\n"
   "ds_read_b64_tr_b16 a[132:133], v117\n"
   "ds_read_b64_tr_b16 a[134:135], v117 offset:6144\n"
   "ds_read_b64_tr_b16 a[136:137], v118\n"
   "ds_read_b64_tr_b16 a[138:139], v118 offset:6144\n"
   "ds_read_b64_tr_b16 a[140:141], v119\n"
   "ds_read_b64_tr_b16 a[142:143], v119 offset:6144\n"
   "s_add_u32 s59, s3, 0x102000\n"
   "buffer_load_dwordx4 v[120:123], v9, s[44:47], s59 offen sc1\n"
   "ds_read_b64_tr_b16 a[144:145], v117 offset:12288\n"
   "ds_read_b64_tr_b16 a[146:147], v117 offset:18432\n"
   "ds_read_b64_tr_b16 a[148:149], v118 offset:12288\n"
   "ds_read_b64_tr_b16 a[150:151], v118 offset:18432\n"
   "ds_read_b64_tr_b16 a[152:153], v119 offset:12288\n"
   "ds_read_b64_tr_b16 a[154:155], v119 offset:18432\n"
   "buffer_load_dwordx4 v[124:127], v9, s[44:47], s59 offen offset:1024 sc1\n"
   "ds_read_b64_tr_b16 a[156:157], v117 offset:24576\n"
   "ds_read_b64_tr_b16 a[158:159], v117 offset:30720\n"
   "ds_read_b64_tr_b16 a[160:161], v118 offset:24576\n"
   "ds_read_b64_tr_b16 a[162:163], v118 offset:30720\n"
   "ds_read_b64_tr_b16 a[164:165], v119 offset:24576\n"
   "ds_read_b64_tr_b16 a[166:167], v119 offset:30720\n"
   "buffer_load_dwordx4 v[128:131], v9, s[44:47], s59 offen offset:2048 sc1\n"
   "ds_read_b64_tr_b16 a[168:169], v117 offset:36864\n"
   "ds_read_b64_tr_b16 a[170:171], v117 offset:43008\n"
   "ds_read_b64_tr_b16 a[172:173], v118 offset:36864\n"
   "ds_read_b64_tr_b16 a[174:175], v118 offset:43008\n"
   "ds_read_b64_tr_b16 a[176:177], v119 offset:36864\n"
   "ds_read_b64_tr_b16 a[178:179], v119 offset:43008\n"
   "buffer_load_dwordx4 v[132:135], v9, s[44:47], s59 offen offset:3072 sc1\n"
   "ds_read_b64_tr_b16 a[180:181], v114 offset:4096\n"
   "ds_read_b64_tr_b16 a[182:183], v114 offset:10240\n"
   "ds_read_b64_tr_b16 a[184:185], v115 offset:4096\n"
   "ds_read_b64_tr_b16 a[186:187], v115 offset:10240\n"
   "ds_read_b64_tr_b16 a[188:189], v116 offset:4096\n"
   "ds_read_b64_tr_b16 a[190:191], v116 offset:10240\n"
   "s_waitcnt lgkmcnt(0)\n"
   "s_barrier\n"
   "s_waitcnt vmcnt(11)\n"
   "v_mfma_f32_16x16x32_bf16 v[152:155], a[192:195], v[32:35], 0\n"
   "s_add_u32 s59, s3, 0x103000\n"
   "v_mfma_f32_16x16x32_bf16 v[156:159], a[196:199], v[32:35], 0\n"
   "buffer_load_dwordx4 v[136:139], v9, s[44:47], s59 offen sc1\n"
   "v_mfma_f32_16x16x32_bf16 v[168:171], a[200:203], v[32:35], 0\n"
   "v_mfma_f32_16x16x32_bf16 v[200:203], a[204:207], v[32:35], 0\n"
   "s_waitcnt vmcnt(11)\n"
   "v_mfma_f32_16x16x32_bf16 v[152:155], a[208:211], v[36:39], v[152:155]\n"
   "ds_read_b64_tr_b16 a[192:193], v172 offset:28672\n"
   "ds_read_b64_tr_b16 a[194:195], v172 offset:29696\n"
   "v_mfma_f32_16x16x32_bf16 v[156:159], a[212:215], v[36:39], v[156:159]\n"
   "ds_read_b64_tr_b16 a[196:197], v173 offset:28672\n"
   "v_mfma_f32_16x16x32_bf16 v[168:171], a[216:219], v[36:39], v[168:171]\n"
   "ds_read_b64_tr_b16 a[198:199], v173 offset:29696\n"
   "buffer_load_dwordx4 v[140:143], v9, s[44:47], s59 offen offset:1024 sc1\n"
   "v_mfma_f32_16x16x32_bf16 v[200:203], a[220:223], v[36:39], v[200:203]\n"
   "s_waitcnt vmcnt(11)\n"
   "ds_read_b64_tr_b16 a[200:201], v175 offset:28672\n"
   "ds_read_b64_tr_b16 a[202:203], v175 offset:29696\n"
   "v_mfma_f32_16x16x32_bf16 v[152:155], a[224:227], v[40:43], v[152:155]\n"
   "ds_read_b64_tr_b16 a[204:205], v176 offset:28672\n"
   "v_mfma_f32_16x16x32_bf16 v[156:159], a[228:231], v[40:43], v[156:159]\n"
   "ds_read_b64_tr_b16 a[206:207], v176 offset:29696\n"
   "v_mfma_f32_16x16x32_bf16 v[168:171], a[232:235], v[40:43], v[168:171]\n"
   "ds_read_b64_tr_b16 a[208:209], v172 offset:32768\n"
   "ds_read_b64_tr_b16 a[210:211], v172 offset:33792\n"
   "v_mfma_f32_16x16x32_bf16 v[200:203], a[236:239], v[40:43], v[200:203]\n"
   "s_waitcnt vmcnt(10)\n"
   "ds_read_b64_tr_b16 a[212:213], v173 offset:32768\n"
   "v_mfma_f32_16x16x32_bf16 v[152:155], a[240:243], v[44:47], v[152:155]\n"
   "ds_read_b64_tr_b16 a[214:215], v173 offset:33792\n"
   "buffer_load_dwordx4 v[144:147], v9, s[44:47], s59 offen offset:2048 sc1\n"
   "v_mfma_f32_16x16x32_bf16 v[156:159], a[244:247], v[44:47], v[156:159]\n"
   "ds_read_b64_tr_b16 a[216:217], v175 offset:32768\n"
   "ds_read_b64_tr_b16 a[218:219], v175 offset:33792\n"
   "v_mfma_f32_16x16x32_bf16 v[168:171], a[248:251], v[44:47], v[168:171]\n"
   "ds_read_b64_tr_b16 a[220:221], v176 offset:32768\n"
   "v_mfma_f32_16x16x32_bf16 v[200:203], a[252:255], v[44:47], v[200:203]\n"
   "s_waitcnt vmcnt(10)\n"
   "ds_read_b64_tr_b16 a[222:223], v176 offset:33792\n"
   "v_mfma_f32_16x16x32_bf16 v[152:155], v[204:207], v[64:67], v[152:155]\n"
   "ds_read_b64_tr_b16 a[224:225], v172 offset:36864\n"
   "ds_read_b64_tr_b16 a[226:227], v172 offset:37888\n"
   "v_mfma_f32_16x16x32_bf16 v[156:159], v[212:215], v[64:67], v[156:159]\n"
   "ds_read_b64_tr_b16 a[228:229], v173 offset:36864\n"
   "buffer_load_dwordx4 v[148:151], v9, s[44:47], s59 offen offset:3072 sc1\n"
   "v_mfma_f32_16x16x32_bf16 v[168:171], v[224:227], v[64:67], v[168:171]\n"
   "ds_read_b64_tr_b16 a[230:231], v173 offset:37888\n"
   "v_mfma_f32_16x16x32_bf16 v[200:203], v[228:231], v[64:67], v[200:203]\n"
   "s_waitcnt vmcnt(10)\n"
   "ds_read_b64_tr_b16 a[232:233], v175 offset:36864\n"
   "ds_read_b64_tr_b16 a[234:235], v175 offset:37888\n"
   "v_mfma_f32_16x16x32_bf16 v[152:155], v[88:91], v[68:71], v[152:155]\n"
   "ds_read_b64_tr_b16 a[236:237], v176 offset:36864\n"
   "v_mfma_f32_16x16x32_bf16 v[156:159], v[92:95], v[68:71], v[156:159]\n"
   "s_sub_u32 s59, 0x180, s28\n"
   "s_and_b32 s59, s59, 0x380\n"
   "ds_read_b64_tr_b16 a[238:239], v176 offset:37888\n"
   "v_mfma_f32_16x16x32_bf16 v[168:171], v[96:99], v[68:71], v[168:171]\n"
   "ds_read_b64_tr_b16 a[240:241], v172 offset:40960\n"
   "ds_read_b64_tr_b16 a[242:243], v172 offset:41984\n"
   "v_mfma_f32_16x16x32_bf16 v[200:203], v[100:103], v[68:71], v[200:203]\n"
   "s_waitcnt vmcnt(9)\n"
   "ds_read_b64_tr_b16 a[244:245], v173 offset:40960\n"
   "v_mfma_f32_16x16x32_bf16 v[152:155], v[104:107], v[72:75], v[152:155]\n"
   "ds_read_b64_tr_b16 a[246:247], v173 offset:41984\n"
   "buffer_load_dword v5, v187, s[28:31], s59 offen\n"
   "v_mfma_f32_16x16x32_bf16 v[156:159], v[108:111], v[72:75], v[156:159]\n"
   "ds_read_b64_tr_b16 a[248:249], v175 offset:40960\n"
   "ds_read_b64_tr_b16 a[250:251], v175 offset:41984\n"
   "v_mfma_f32_16x16x32_bf16 v[168:171], v[80:83], v[72:75], v[168:171]\n"
   "s_add_u32 m0, s90, 0x18000\n"
   "s_add_u32 s58, s91, 0\n"
   "ds_read_b64_tr_b16 a[252:253], v176 offset:40960\n"
   "v_mfma_f32_16x16x32_bf16 v[200:203], v[84:87], v[72:75], v[200:203]\n"
   "s_waitcnt lgkmcnt(14)\n"
   "ds_read_b64_tr_b16 a[254:255], v176 offset:41984\n"
   "s_waitcnt vmcnt(9)\n"
   "v_mfma_f32_16x16x32_bf16 v[152:155], a[192:195], v[76:79], v[152:155]\n"
   "ds_read_b64_tr_b16 v[204:205], v172 offset:45056\n"
   "ds_read_b64_tr_b16 v[206:207], v172 offset:46080\n"
   "v_mfma_f32_16x16x32_bf16 v[156:159], a[196:199], v[76:79], v[156:159]\n"
   "ds_read_b64_tr_b16 v[212:213], v173 offset:45056\n"
   "buffer_load_dwordx4 v11, s[28:31], s58 offen lds\n"
   "v_mfma_f32_16x16x32_bf16 v[168:171], a[200:203], v[76:79], v[168:171]\n"
   "ds_read_b64_tr_b16 v[214:215], v173 offset:46080\n"
   "v_mfma_f32_16x16x32_bf16 v[200:203], a[204:207], v[76:79], v[200:203]\n"
   "s_waitcnt vmcnt(9)\n"
   "ds_read_b64_tr_b16 v[224:225], v175 offset:45056\n"
   "ds_read_b64_tr_b16 v[226:227], v175 offset:46080\n"
   "v_mfma_f32_16x16x32_bf16 v[152:155], a[208:211], v[120:123], v[152:155]\n"
   "ds_read_b64_tr_b16 v[228:229], v176 offset:45056\n"
   "v_mfma_f32_16x16x32_bf16 v[156:159], a[212:215], v[120:123], v[156:159]\n"
   "ds_read_b64_tr_b16 v[230:231], v176 offset:46080\n"
   "v_mfma_f32_16x16x32_bf16 v[168:171], a[216:219], v[120:123], v[168:171]\n"
   "ds_read_b64_tr_b16 v[88:89], v172 offset:49152\n"
   "ds_read_b64_tr_b16 v[90:91], v172 offset:50176\n"
   "buffer_load_dwordx4 v12, s[28:31], s58 offen offset:1024 lds\n"
   "v_mfma_f32_16x16x32_bf16 v[200:203], a[220:223], v[120:123], v[200:203]\n"
   "s_waitcnt lgkmcnt(14)\n"
   "ds_read_b64_tr_b16 v[92:93], v173 offset:49152\n"
   "s_waitcnt vmcnt(9)\n"
   "v_mfma_f32_16x16x32_bf16 v[152:155], a[224:227], v[124:127], v[152:155]\n"
   "ds_read_b64_tr_b16 v[94:95], v173 offset:50176\n"
   "v_mfma_f32_16x16x32_bf16 v[156:159], a[228:231], v[124:127], v[156:159]\n"
   "ds_read_b64_tr_b16 v[96:97], v175 offset:49152\n"
   "ds_read_b64_tr_b16 v[98:99], v175 offset:50176\n"
   "v_mfma_f32_16x16x32_bf16 v[168:171], a[232:235], v[124:127], v[168:171]\n"
   "ds_read_b64_tr_b16 v[100:101], v176 offset:49152\n"
   "v_mfma_f32_16x16x32_bf16 v[200:203], a[236:239], v[124:127], v[200:203]\n"
   "s_waitcnt lgkmcnt(14)\n"
   "ds_read_b64_tr_b16 v[102:103], v176 offset:50176\n"
   "s_waitcnt vmcnt(8)\n"
   "v_mfma_f32_16x16x32_bf16 v[152:155], a[240:243], v[128:131], v[152:155]\n"
   "ds_read_b64_tr_b16 v[104:105], v172 offset:53248\n"
   "ds_read_b64_tr_b16 v[106:107], v172 offset:54272\n"
   "buffer_load_dwordx4 v13, s[28:31], s58 offen offset:2048 lds\n"
   "v_mfma_f32_16x16x32_bf16 v[156:159], a[244:247], v[128:131], v[156:159]\n"
   "ds_read_b64_tr_b16 v[108:109], v173 offset:53248\n"
   "v_mfma_f32_16x16x32_bf16 v[168:171], a[248:251], v[128:131], v[168:171]\n"
   "ds_read_b64_tr_b16 v[110:111], v173 offset:54272\n"
   "v_mfma_f32_16x16x32_bf16 v[200:203], a[252:255], v[128:131], v[200:203]\n"
   "s_waitcnt lgkmcnt(12)\n"
   "ds_read_b64_tr_b16 v[80:81], v175 offset:53248\n"
   "ds_read_b64_tr_b16 v[82:83], v175 offset:54272\n"
   "s_waitcnt vmcnt(8)\n"
   "v_mfma_f32_16x16x32_bf16 v[152:155], v[204:207], v[132:135], v[152:155]\n"
   "ds_read_b64_tr_b16 v[84:85], v176 offset:53248\n"
   "v_mfma_f32_16x16x32_bf16 v[156:159], v[212:215], v[132:135], v[156:159]\n"
   "ds_read_b64_tr_b16 v[86:87], v176 offset:54272\n"
   "buffer_load_dwordx4 v174, s[28:31], s58 offen offset:3072 lds\n"
   "v_mfma_f32_16x16x32_bf16 v[168:171], v[224:227], v[132:135], v[168:171]\n"
   "ds_read_b64_tr_b16 a[192:193], v172 offset:57344\n"
   "ds_read_b64_tr_b16 a[194:195], v172 offset:58368\n"
   "v_mfma_f32_16x16x32_bf16 v[200:203], v[228:231], v[132:135], v[200:203]\n"
   "s_waitcnt lgkmcnt(10)\n"
   "ds_read_b64_tr_b16 a[196:197], v173 offset:57344\n"
   "ds_read_b64_tr_b16 a[198:199], v173 offset:58368\n"
   "s_waitcnt vmcnt(8)\n"
   "v_mfma_f32_16x16x32_bf16 v[152:155], v[88:91], v[136:139], v[152:155]\n"
   "ds_read_b64_tr_b16 a[200:201], v175 offset:57344\n"
   "v_mfma_f32_16x16x32_bf16 v[156:159], v[92:95], v[136:139], v[156:159]\n"
   "s_add_u32 m0, s90, 0x19000\n"
   "s_add_u32 s58, s91, 0x8000\n"
   "ds_read_b64_tr_b16 a[202:203], v175 offset:58368\n"
   "v_mfma_f32_16x16x32_bf16 v[168:171], v[96:99], v[136:139], v[168:171]\n"
   "ds_read_b64_tr_b16 a[204:205], v176 offset:57344\n"
   "ds_read_b64_tr_b16 a[206:207], v176 offset:58368\n"
   "v_mfma_f32_16x16x32_bf16 v[200:203], v[100:103], v[136:139], v[200:203]\n"
   "s_waitcnt lgkmcnt(8)\n"
   "ds_read_b64_tr_b16 a[208:209], v172 offset:61440\n"
   "ds_read_b64_tr_b16 a[210:211], v172 offset:62464\n"
   "s_waitcnt vmcnt(7)\n"
   "v_mfma_f32_16x16x32_bf16 v[152:155], v[104:107], v[140:143], v[152:155]\n"
   "ds_read_b64_tr_b16 a[212:213], v173 offset:61440\n"
   "ds_read_b64_tr_b16 a[214:215], v173 offset:62464\n"
   "buffer_load_dwordx4 v11, s[28:31], s58 offen lds\n"
   "v_mfma_f32_16x16x32_bf16 v[156:159], v[108:111], v[140:143], v[156:159]\n"
   "ds_read_b64_tr_b16 a[216:217], v175 offset:61440\n"
   "ds_read_b64_tr_b16 a[218:219], v175 offset:62464\n"
   "v_mfma_f32_16x16x32_bf16 v[168:171], v[80:83], v[140:143], v[168:171]\n"
   "ds_read_b64_tr_b16 a[220:221], v176 offset:61440\n"
   "ds_read_b64_tr_b16 a[222:223], v176 offset:62464\n"
   "v_mfma_f32_16x16x32_bf16 v[200:203], v[84:87], v[140:143], v[200:203]\n"
   "s_waitcnt lgkmcnt(8)\n"
   "s_waitcnt vmcnt(7)\n"
   "v_mfma_f32_16x16x32_bf16 v[152:155], a[192:195], v[144:147], v[152:155]\n"
   "buffer_load_dwordx4 v12, s[28:31], s58 offen offset:1024 lds\n"
   "v_mfma_f32_16x16x32_bf16 v[156:159], a[196:199], v[144:147], v[156:159]\n"
   "v_mfma_f32_16x16x32_bf16 v[168:171], a[200:203], v[144:147], v[168:171]\n"
   "v_mfma_f32_16x16x32_bf16 v[200:203], a[204:207], v[144:147], v[200:203]\n"
   "s_waitcnt lgkmcnt(0)\n"
   "s_waitcnt vmcnt(7)\n"
   "v_mfma_f32_16x16x32_bf16 v[152:155], a[208:211], v[148:151], v[152:155]\n"
   "v_mfma_f32_16x16x32_bf16 v[156:159], a[212:215], v[148:151], v[156:159]\n"
   "v_mfma_f32_16x16x32_bf16 v[168:171], a[216:219], v[148:151], v[168:171]\n"
   "v_mfma_f32_16x16x32_bf16 v[200:203], a[220:223], v[148:151], v[200:203]\n"
   "s_nop 4\n"
   "v_pk_fma_f32 v[48:49], v[152:153], v[16:17], v[48:49]\n"
   "v_pk_fma_f32 v[50:51], v[154:155], v[18:19], v[50:51]\n"
   "v_pk_fma_f32 v[52:53], v[156:157], v[20:21], v[52:53]\n"
   "v_pk_fma_f32 v[54:55], v[158:159], v[22:23], v[54:55]\n"
   "v_pk_mul_f32 v[152:153], v[48:49], v[192:193]\n"
   "v_pk_mul_f32 v[154:155], v[50:51], v[194:195]\n"
   "v_pk_mul_f32 v[156:157], v[52:53], v[196:197]\n"
   "v_pk_mul_f32 v[158:159], v[54:55], v[198:199]\n"
   "v_cvt_pk_bf16_f32 v32, v152, v153\n"
   "v_cvt_pk_bf16_f32 v33, v154, v155\n"
   "v_cvt_pk_bf16_f32 v34, v156, v157\n"
   "v_cvt_pk_bf16_f32 v35, v158, v159\n"
   "buffer_store_dwordx4 v[32:35], v9, s[44:47], s67 offen sc1\n"
   "v_pk_fma_f32 v[56:57], v[168:169], v[24:25], v[56:57]\n"
   "v_pk_fma_f32 v[58:59], v[170:171], v[26:27], v[58:59]\n"
   "v_pk_fma_f32 v[60:61], v[200:201], v[28:29], v[60:61]\n"
   "v_pk_fma_f32 v[62:63], v[202:203], v[30:31], v[62:63]\n"
   "v_pk_mul_f32 v[168:169], v[56:57], v[216:217]\n"
   "v_pk_mul_f32 v[170:171], v[58:59], v[218:219]\n"
   "v_pk_mul_f32 v[200:201], v[60:61], v[220:221]\n"
   "v_pk_mul_f32 v[202:203], v[62:63], v[222:223]\n"
   "v_cvt_pk_bf16_f32 v36, v168, v169\n"
   "v_cvt_pk_bf16_f32 v37, v170, v171\n"
   "v_cvt_pk_bf16_f32 v38, v200, v201\n"
   "v_cvt_pk_bf16_f32 v39, v202, v203\n"
   "buffer_store_dwordx4 v[36:39], v9, s[44:47], s67 offen offset:1024 sc1\n"
   "buffer_load_dwordx4 v13, s[28:31], s58 offen offset:2048 lds\n"
   "buffer_load_dwordx4 v174, s[28:31], s58 offen offset:3072 lds\n"
   "s_add_u32 m0, s90, 0x1a000\n"
   "s_add_u32 s58, s91, 0x10000\n"
   "buffer_load_dwordx4 v11, s[28:31], s58 offen lds\n"
   "buffer_load_dwordx4 v12, s[28:31], s58 offen offset:1024 lds\n"
   "v_pk_add_f32 v[16:17], v[48:49], v[50:51]\n"
   "v_pk_mul_f32 v[18:19], v[48:49], v[48:49]\n"
   "v_pk_fma_f32 v[18:19], v[50:51], v[50:51], v[18:19]\n"
   "v_pk_add_f32 v[16:17], v[16:17], v[52:53]\n"
   "v_pk_fma_f32 v[18:19], v[52:53], v[52:53], v[18:19]\n"
   "v_pk_add_f32 v[16:17], v[16:17], v[54:55]\n"
   "v_pk_fma_f32 v[18:19], v[54:55], v[54:55], v[18:19]\n"
   "v_pk_add_f32 v[16:17], v[16:17], v[56:57]\n"
   "v_pk_fma_f32 v[18:19], v[56:57], v[56:57], v[18:19]\n"
   "v_pk_add_f32 v[16:17], v[16:17], v[58:59]\n"
   "v_pk_fma_f32 v[18:19], v[58:59], v[58:59], v[18:19]\n"
   "v_pk_add_f32 v[16:17], v[16:17], v[60:61]\n"
   "v_pk_fma_f32 v[18:19], v[60:61], v[60:61], v[18:19]\n"
   "v_pk_add_f32 v[16:17], v[16:17], v[62:63]\n"
   "v_pk_fma_f32 v[18:19], v[62:63], v[62:63], v[18:19]\n"
   "v_add_f32 v20, v16, v17\n"
   "v_add_f32 v22, v18, v19\n"
   "v_mov_b32 v1, v20\n"
   "v_mov_b32 v2, v22\n"
   "s_nop 0\n"
   "v_permlane16_swap_b32 v20, v1\n"
   "v_permlane16_swap_b32 v22, v2\n"
   "v_add_f32 v20, v20, v1\n"
   "v_add_f32 v22, v22, v2\n"
   "v_mov_b32 v1, v20\n"
   "v_mov_b32 v2, v22\n"
   "s_nop 0\n"
   "v_permlane32_swap_b32 v20, v1\n"
   "v_permlane32_swap_b32 v22, v2\n"
   "v_add_f32 v20, v20, v1\n"
   "v_add_f32 v22, v22, v2\n"
   "v_mov_b32_e64 v21, s48\n"
   "v_mov_b32 v23, s48\n"
   "s_lshl_b32 s57, s51, 8\n"
   "s_lshl3_add_u32 s56, s72, s57\n"
   "s_add_u32 s56, s56, 0x80000\n"
   "s_mov_b64 exec, 0xffff\n"
   "buffer_store_dwordx4 v[20:23], v9, s[44:47], s56 offen sc1\n"
   "s_mov_b64 exec, -1\n"
   "ds_read_b64 v[80:81], v6 offset:576\n"
   "ds_read_b64 v[82:83], v6 offset:608\n"
   "ds_read_b64 v[84:85], v6 offset:640\n"
   "ds_read_b64 v[86:87], v6 offset:672\n"
   "ds_read_b64 v[88:89], v6 offset:704\n"
   "ds_read_b64 v[90:91], v6 offset:736\n"
   "ds_read_b64 v[92:93], v6 offset:768\n"
   "ds_read_b64 v[94:95], v6 offset:800\n"
   "ds_read_b64 v[96:97], v6 offset:832\n"
   "ds_read_b64 v[98:99], v6 offset:864\n"
   "ds_read_b64 v[100:101], v6 offset:896\n"
   "ds_read_b64 v[102:103], v6 offset:928\n"
   "ds_read_b64 v[104:105], v6 offset:960\n"
   "ds_read_b64 v[106:107], v6 offset:992\n"
   "s_waitcnt lgkmcnt(12)\n"
   "v_mfma_f32_16x16x32_bf16 v[232:235], a[108:111], v[80:83], v[232:235]\n"
   "s_and_b32 s66, s51, 1\n"
   "s_lshr_b32 s12, s51, 1\n"
   "v_mfma_f32_16x16x32_bf16 v[236:239], a[112:115], v[80:83], v[236:239]\n"
   "s_lshl_b32 s57, s12, 6\n"
   "s_add_u32 s56, s90, 0x18000\n"
   "v_mfma_f32_16x16x32_bf16 v[240:243], a[116:119], v[80:83], v[240:243]\n"
   "s_waitcnt lgkmcnt(10)\n"
   "v_xor_b32 v172, s57, v172\n"
   "v_mfma_f32_16x16x32_bf16 v[232:235], a[120:123], v[84:87], v[232:235]\n"
   "v_add_u32 v172, s56, v172\n"
   "v_xor_b32 v173, s57, v173\n"
   "v_mfma_f32_16x16x32_bf16 v[236:239], a[124:127], v[84:87], v[236:239]\n"
   "v_add_u32 v173, s56, v173\n"
   "v_xor_b32 v175, s57, v175\n"
   "v_mfma_f32_16x16x32_bf16 v[240:243], a[128:131], v[84:87], v[240:243]\n"
   "s_waitcnt lgkmcnt(8)\n"
   "v_add_u32 v175, s56, v175\n"
   "v_mfma_f32_16x16x32_bf16 v[232:235], a[132:135], v[88:91], v[232:235]\n"
   "v_xor_b32 v176, s57, v176\n"
   "v_add_u32 v176, s56, v176\n"
   "v_mfma_f32_16x16x32_bf16 v[236:239], a[136:139], v[88:91], v[236:239]\n"
   "s_add_u32 s57, s57, 0x10880\n"
   "v_lshl_add_u32 v2, v8, 3, s57\n"
   "v_mfma_f32_16x16x32_bf16 v[240:243], a[140:143], v[88:91], v[240:243]\n"
   "s_waitcnt lgkmcnt(6)\n"
   "ds_read_b64 v[32:33], v2\n"
   "v_mfma_f32_16x16x32_bf16 v[232:235], a[144:147], v[92:95], v[232:235]\n"
   "ds_read_b64 v[34:35], v2 offset:32\n"
   "v_and_b32 v1, 7, v7\n"
   "v_mfma_f32_16x16x32_bf16 v[236:239], a[148:151], v[92:95], v[236:239]\n"
   "v_lshlrev_b32_e64 v1, 2, v1\n"
   "v_lshrrev_b32_e64 v1, v1, s48\n"
   "v_mfma_f32_16x16x32_bf16 v[240:243], a[152:155], v[92:95], v[240:243]\n"
   "s_waitcnt lgkmcnt(6)\n"
   "s_lshl_b32 s56, s48, 31\n"
   "v_mfma_f32_16x16x32_bf16 v[232:235], a[156:159], v[96:99], v[232:235]\n"
   "v_bfe_u32 v14, v1, 0, 1\n"
   "v_lshl_or_b32 v14, v14, 15, s56\n"
   "v_mfma_f32_16x16x32_bf16 v[236:239], a[160:163], v[96:99], v[236:239]\n"
   "s_xor_b32 s56, s56, 0x80000000\n"
   "v_bfe_u32 v15, v1, 1, 1\n"
   "v_mfma_f32_16x16x32_bf16 v[240:243], a[164:167], v[96:99], v[240:243]\n"
   "s_waitcnt lgkmcnt(4)\n"
   "v_lshl_or_b32 v15, v15, 15, s56\n"
   "v_mfma_f32_16x16x32_bf16 v[232:235], a[168:171], v[100:103], v[232:235]\n"
   "s_xor_b32 s56, s56, 0x80000000\n"
   "v_bfe_u32 v114, v1, 2, 1\n"
   "v_mfma_f32_16x16x32_bf16 v[236:239], a[172:175], v[100:103], v[236:239]\n"
   "v_lshl_or_b32 v114, v114, 15, s56\n"
   "s_xor_b32 s56, s56, 0x80000000\n"
   "v_mfma_f32_16x16x32_bf16 v[240:243], a[176:179], v[100:103], v[240:243]\n"
   "s_waitcnt lgkmcnt(2)\n"
   "v_bfe_u32 v115, v1, 3, 1\n"
   "v_mfma_f32_16x16x32_bf16 v[232:235], a[180:183], v[104:107], v[232:235]\n"
   "v_lshl_or_b32 v115, v115, 15, s56\n"
   "v_add_u32_e64 v3, s61, v9\n"
   "v_mfma_f32_16x16x32_bf16 v[236:239], a[184:187], v[104:107], v[236:239]\n"
   "s_lshl1_add_u32 s56, s66, s12\n"
   "s_lshl_b32 s56, s56, 11\n"
   "v_mfma_f32_16x16x32_bf16 v[240:243], a[188:191], v[104:107], v[240:243]\n"
   "v_add_u32 v4, s56, v9\n"
   "s_waitcnt vmcnt(5)\n"
   "s_waitcnt lgkmcnt(0)\n"
   "s_barrier\n"
   "buffer_store_dwordx4 v[208:211], v188, s[44:47], s75 offen sc1\n"
   "s_add_u32 s75, s75, 0x8000\n"
   ".Lfb_pc29:\n"
   "s_lshr_b32 s60, s2, 3\n"
   "s_lshl_b32 s60, s60, 7\n"
   "s_add_u32 s60, s60, .Lfb_pc29-.Lfb_pc\n"
   "v_lshlrev_b32_e64 v5, 12, v0\n"
   "buffer_load_dword v5, v5, s[4:7], s60 offen\n"
   "ds_write_b128 v3, v[48:51]\n"
   "ds_write_b128 v3, v[52:55] offset:1024\n"
   "ds_write_b128 v3, v[56:59] offset:2048\n"
   "ds_write_b128 v3, v[60:63] offset:3072\n"
   "v_mov_b32_dpp v36, v232 quad_perm:[1,1,3,3] row_mask:0xf bank_mask:0xf\n"
   "v_mov_b32_dpp v37, v233 quad_perm:[1,1,3,3] row_mask:0xf bank_mask:0xf\n"
   "v_mov_b32_dpp v38, v234 quad_perm:[1,1,3,3] row_mask:0xf bank_mask:0xf\n"
   "v_mov_b32_dpp v39, v235 quad_perm:[1,1,3,3] row_mask:0xf bank_mask:0xf\n"
   "v_mov_b32_dpp v40, v236 quad_perm:[1,1,3,3] row_mask:0xf bank_mask:0xf\n"
   "v_mov_b32_dpp v41, v237 quad_perm:[1,1,3,3] row_mask:0xf bank_mask:0xf\n"
   "v_mov_b32_dpp v42, v238 quad_perm:[1,1,3,3] row_mask:0xf bank_mask:0xf\n"
   "v_mov_b32_dpp v43, v239 quad_perm:[1,1,3,3] row_mask:0xf bank_mask:0xf\n"
   "v_mov_b32_dpp v44, v240 quad_perm:[1,1,3,3] row_mask:0xf bank_mask:0xf\n"
   "v_mov_b32_dpp v45, v241 quad_perm:[1,1,3,3] row_mask:0xf bank_mask:0xf\n"
   "v_mov_b32_dpp v46, v242 quad_perm:[1,1,3,3] row_mask:0xf bank_mask:0xf\n"
   "v_mov_b32_dpp v47, v243 quad_perm:[1,1,3,3] row_mask:0xf bank_mask:0xf\n"
   "v_mov_b32_dpp v232, v232 quad_perm:[0,0,2,2] row_mask:0xf bank_mask:0xf\n"
   "v_mov_b32_dpp v233, v233 quad_perm:[0,0,2,2] row_mask:0xf bank_mask:0xf\n"
   "v_mov_b32_dpp v234, v234 quad_perm:[0,0,2,2] row_mask:0xf bank_mask:0xf\n"
   "v_mov_b32_dpp v235, v235 quad_perm:[0,0,2,2] row_mask:0xf bank_mask:0xf\n"
   "v_mov_b32_dpp v236, v236 quad_perm:[0,0,2,2] row_mask:0xf bank_mask:0xf\n"
   "v_mov_b32_dpp v237, v237 quad_perm:[0,0,2,2] row_mask:0xf bank_mask:0xf\n"
   "v_mov_b32_dpp v238, v238 quad_perm:[0,0,2,2] row_mask:0xf bank_mask:0xf\n"
   "v_mov_b32_dpp v239, v239 quad_perm:[0,0,2,2] row_mask:0xf bank_mask:0xf\n"
   "v_mov_b32_dpp v240, v240 quad_perm:[0,0,2,2] row_mask:0xf bank_mask:0xf\n"
   "v_mov_b32_dpp v241, v241 quad_perm:[0,0,2,2] row_mask:0xf bank_mask:0xf\n"
   "v_mov_b32_dpp v242, v242 quad_perm:[0,0,2,2] row_mask:0xf bank_mask:0xf\n"
   "v_mov_b32_dpp v243, v243 quad_perm:[0,0,2,2] row_mask:0xf bank_mask:0xf\n"
   "s_nop 15\n"
   "s_nop 15\n"
   "s_nop 15\n"
   "s_nop 15\n"
   "s_nop 15\n"
   "s_nop 15\n"
   "buffer_load_dwordx2 v[166:167], v112, s[44:47], s76 offen sc1\n"
   "buffer_load_dwordx4 v13, s[28:31], s58 offen offset:2048 lds\n"
   "buffer_load_dwordx4 v174, s[28:31], s58 offen offset:3072 lds\n"
   "s_add_u32 m0, s90, 0x1b000\n"
   "s_add_u32 s58, s91, 0x18000\n"
   "buffer_load_dwordx4 v11, s[28:31], s58 offen lds\n"
   "buffer_load_dwordx4 v12, s[28:31], s58 offen offset:1024 lds\n"
   "buffer_load_dwordx4 v13, s[28:31], s58 offen offset:2048 lds\n"
   "buffer_load_dwordx4 v174, s[28:31], s58 offen offset:3072 lds\n"
   "s_waitcnt lgkmcnt(0)\n"
   "s_barrier\n"
   "ds_read_b128 v[48:51], v4\n"
   "ds_read_b128 v[52:55], v4 offset:1024\n"
   "ds_read_b128 v[56:59], v4 offset:8192\n"
   "ds_read_b128 v[60:63], v4 offset:9216\n"
   "ds_read_b64_tr_b16 a[192:193], v172\n"
   "ds_read_b64_tr_b16 a[194:195], v172 offset:1024\n"
   "ds_read_b64_tr_b16 a[196:197], v173\n"
   "ds_read_b64_tr_b16 a[198:199], v173 offset:1024\n"
   "ds_read_b64_tr_b16 a[200:201], v175\n"
   "ds_read_b64_tr_b16 a[202:203], v175 offset:1024\n"
   "ds_read_b64_tr_b16 a[204:205], v176\n"
   "ds_read_b64_tr_b16 a[206:207], v176 offset:1024\n"
   "s_waitcnt vmcnt(12)\n"
   "s_nop 0\n"
   "ds_read_b64_tr_b16 a[208:209], v172 offset:4096\n"
   "ds_read_b64_tr_b16 a[210:211], v172 offset:5120\n"
   "ds_read_b64_tr_b16 a[212:213], v173 offset:4096\n"
   "ds_read_b64_tr_b16 a[214:215], v173 offset:5120\n"
   "ds_read_b64_tr_b16 a[216:217], v175 offset:4096\n"
   "ds_read_b64_tr_b16 a[218:219], v175 offset:5120\n"
   "ds_read_b64_tr_b16 a[220:221], v176 offset:4096\n"
   "ds_read_b64_tr_b16 a[222:223], v176 offset:5120\n"
   "s_add_u32 m0, s90, 0x18000\n"
   "s_add_u32 s58, s91, 0x20000\n"
   "s_waitcnt lgkmcnt(8)\n"
   "buffer_load_dwordx4 v11, s[28:31], s58 offen lds\n"
   "buffer_load_dwordx4 v12, s[28:31], s58 offen offset:1024 lds\n"
   "buffer_load_dwordx4 v13, s[28:31], s58 offen offset:2048 lds\n"
   "buffer_load_dwordx4 v174, s[28:31], s58 offen offset:3072 lds\n"
   "s_lshl2_add_u32 s13, s66, s12\n"
   "s_lshl_b32 s13, s13, 12\n"
   "s_add_u32 s59, s13, s70\n"
   "v_lshlrev_b32_e64 v6, 10, v8\n"
   "v_lshl_or_b32 v6, v7, 4, v6\n"
   "s_lshr_b32 s56, s70, 3\n"
   "s_lshl_b32 s57, s51, 8\n"
   "s_add_u32 s56, s56, s57\n"
   "s_add_u32 s14, s56, 0x80000\n"
   "s_add_u32 s15, s14, 0x1000\n"
   "s_waitcnt vmcnt(10)\n"
   "v_cmp_ne_u64 vcc, s[48:49], v[166:167]\n"
   "s_nop 0\n"
   "s_and_b64 vcc, vcc, s[88:89]\n"
   "s_cbranch_vccnz .Lfb_stub30\n"
   ".Lfb_back31:\n"
   "s_add_u32 s76, s76, 0x8000\n"
   "s_waitcnt lgkmcnt(0)\n"
   "s_barrier\n"
   "s_add_u32 m0, s13, 0\n"
   "s_add_u32 s58, s59, 0x300000\n"
   "buffer_load_dwordx4 v9, s[44:47], s58 offen sc1 lds\n"
   "buffer_load_dwordx4 v9, s[44:47], s58 offen offset:1024 sc1 lds\n"
   "buffer_load_dwordx4 v9, s[44:47], s58 offen offset:2048 sc1 lds\n"
   "buffer_load_dwordx4 v9, s[44:47], s58 offen offset:3072 sc1 lds\n"
   "s_add_u32 m0, s13, 0x2000\n"
   "s_add_u32 s58, s59, 0x302000\n"
   "buffer_load_dwordx4 v9, s[44:47], s58 offen sc1 lds\n"
   "buffer_load_dwordx4 v9, s[44:47], s58 offen offset:1024 sc1 lds\n"
   "buffer_load_dwordx4 v9, s[44:47], s58 offen offset:2048 sc1 lds\n"
   "buffer_load_dwordx4 v9, s[44:47], s58 offen offset:3072 sc1 lds\n"
   "buffer_load_dwordx4 v[16:19], v6, s[44:47], s14 offen sc1\n"
   "buffer_load_dwordx4 v[20:23], v6, s[44:47], s15 offen sc1\n"
   "s_waitcnt vmcnt(18)\n"
   "ds_read_b64_tr_b16 a[224:225], v172 offset:8192\n"
   "ds_read_b64_tr_b16 a[226:227], v172 offset:9216\n"
   "ds_read_b64_tr_b16 a[228:229], v173 offset:8192\n"
   "ds_read_b64_tr_b16 a[230:231], v173 offset:9216\n"
   "ds_read_b64_tr_b16 a[232:233], v175 offset:8192\n"
   "ds_read_b64_tr_b16 a[234:235], v175 offset:9216\n"
   "ds_read_b64_tr_b16 a[236:237], v176 offset:8192\n"
   "ds_read_b64_tr_b16 a[238:239], v176 offset:9216\n"
   "s_waitcnt vmcnt(14)\n"
   "ds_read_b64_tr_b16 a[240:241], v172 offset:12288\n"
   "ds_read_b64_tr_b16 a[242:243], v172 offset:13312\n"
   "ds_read_b64_tr_b16 a[244:245], v173 offset:12288\n"
   "ds_read_b64_tr_b16 a[246:247], v173 offset:13312\n"
   "ds_read_b64_tr_b16 a[248:249], v175 offset:12288\n"
   "ds_read_b64_tr_b16 a[250:251], v175 offset:13312\n"
   "ds_read_b64_tr_b16 a[252:253], v176 offset:12288\n"
   "ds_read_b64_tr_b16 a[254:255], v176 offset:13312\n"
   "s_waitcnt vmcnt(6)\n"
   "s_barrier\n"
   "ds_read_b128 v[80:83], v9\n"
   "ds_read_b128 v[84:87], v9 offset:16384\n"
   "ds_read_b128 v[88:91], v9 offset:1024\n"
   "ds_read_b128 v[92:95], v9 offset:17408\n"
   "s_waitcnt lgkmcnt(2)\n"
   "v_mfma_f32_16x16x32_bf16 v[64:67], a[0:3], v[80:83], 0\n"
   "s_add_u32 m0, s13, 0x8000\n"
   "s_add_u32 s58, s59, 0x308000\n"
   "ds_read_b128 v[96:99], v9 offset:2048\n"
   "v_mfma_f32_16x16x32_bf16 v[68:71], a[4:7], v[80:83], 0\n"
   "buffer_load_dwordx4 v9, s[44:47], s58 offen sc1 lds\n"
   "v_mfma_f32_16x16x32_bf16 v[72:75], a[8:11], v[80:83], 0\n"
   "ds_read_b128 v[100:103], v9 offset:18432\n"
   "v_mfma_f32_16x16x32_bf16 v[76:79], a[0:3], v[84:87], 0\n"
   "v_mfma_f32_16x16x32_bf16 v[116:119], a[4:7], v[84:87], 0\n"
   "buffer_load_dwordx4 v9, s[44:47], s58 offen offset:1024 sc1 lds\n"
   "v_mfma_f32_16x16x32_bf16 v[120:123], a[8:11], v[84:87], 0\n"
   "s_waitcnt lgkmcnt(2)\n"
   "v_mfma_f32_16x16x32_bf16 v[64:67], a[12:15], v[88:91], v[64:67]\n"
   "ds_read_b128 v[80:83], v9 offset:3072\n"
   "v_mfma_f32_16x16x32_bf16 v[68:71], a[16:19], v[88:91], v[68:71]\n"
   "v_mfma_f32_16x16x32_bf16 v[72:75], a[20:23], v[88:91], v[72:75]\n"
   "ds_read_b128 v[84:87], v9 offset:19456\n"
   "v_mfma_f32_16x16x32_bf16 v[76:79], a[12:15], v[92:95], v[76:79]\n"
   "buffer_load_dwordx4 v9, s[44:47], s58 offen offset:2048 sc1 lds\n"
   "v_mfma_f32_16x16x32_bf16 v[116:119], a[16:19], v[92:95], v[116:119]\n"
   "v_mfma_f32_16x16x32_bf16 v[120:123], a[20:23], v[92:95], v[120:123]\n"
   "s_waitcnt lgkmcnt(2)\n"
   "v_mfma_f32_16x16x32_bf16 v[64:67], a[24:27], v[96:99], v[64:67]\n"
   "ds_read_b128 v[88:91], v9 offset:4096\n"
   "v_mfma_f32_16x16x32_bf16 v[68:71], a[28:31], v[96:99], v[68:71]\n"
   "buffer_load_dwordx4 v9, s[44:47], s58 offen offset:3072 sc1 lds\n"
   "v_mfma_f32_16x16x32_bf16 v[72:75], a[32:35], v[96:99], v[72:75]\n"
   "ds_read_b128 v[92:95], v9 offset:20480\n"
   "v_mfma_f32_16x16x32_bf16 v[76:79], a[24:27], v[100:103], v[76:79]\n"
   "v_mfma_f32_16x16x32_bf16 v[116:119], a[28:31], v[100:103], v[116:119]\n"
   "s_add_u32 m0, s13, 0xa000\n"
   "s_add_u32 s58, s59, 0x30a000\n"
   "v_mfma_f32_16x16x32_bf16 v[120:123], a[32:35], v[100:103], v[120:123]\n"
   "s_waitcnt lgkmcnt(2)\n"
   "v_mfma_f32_16x16x32_bf16 v[64:67], a[36:39], v[80:83], v[64:67]\n"
   "ds_read_b128 v[96:99], v9 offset:5120\n"
   "v_mfma_f32_16x16x32_bf16 v[68:71], a[40:43], v[80:83], v[68:71]\n"
   "buffer_load_dwordx4 v9, s[44:47], s58 offen sc1 lds\n"
   "v_mfma_f32_16x16x32_bf16 v[72:75], a[44:47], v[80:83], v[72:75]\n"
   "ds_read_b128 v[100:103], v9 offset:21504\n"
   "v_mfma_f32_16x16x32_bf16 v[76:79], a[36:39], v[84:87], v[76:79]\n"
   "v_mfma_f32_16x16x32_bf16 v[116:119], a[40:43], v[84:87], v[116:119]\n"
   "buffer_load_dwordx4 v9, s[44:47], s58 offen offset:1024 sc1 lds\n"
   "v_mfma_f32_16x16x32_bf16 v[120:123], a[44:47], v[84:87], v[120:123]\n"
   "s_waitcnt lgkmcnt(2)\n"
   "v_mfma_f32_16x16x32_bf16 v[64:67], a[48:51], v[88:91], v[64:67]\n"
   "ds_read_b128 v[80:83], v9 offset:6144\n"
   "v_mfma_f32_16x16x32_bf16 v[68:71], a[52:55], v[88:91], v[68:71]\n"
   "v_mfma_f32_16x16x32_bf16 v[72:75], a[56:59], v[88:91], v[72:75]\n"
   "ds_read_b128 v[84:87], v9 offset:22528\n"
   "v_mfma_f32_16x16x32_bf16 v[76:79], a[48:51], v[92:95], v[76:79]\n"
   "buffer_load_dwordx4 v9, s[44:47], s58 offen offset:2048 sc1 lds\n"
   "v_mfma_f32_16x16x32_bf16 v[116:119], a[52:55], v[92:95], v[116:119]\n"
   "v_mfma_f32_16x16x32_bf16 v[120:123], a[56:59], v[92:95], v[120:123]\n"
   "s_waitcnt lgkmcnt(2)\n"
   "v_mfma_f32_16x16x32_bf16 v[64:67], a[60:63], v[96:99], v[64:67]\n"
   "ds_read_b128 v[88:91], v9 offset:7168\n"
   "v_mfma_f32_16x16x32_bf16 v[68:71], a[64:67], v[96:99], v[68:71]\n"
   "buffer_load_dwordx4 v9, s[44:47], s58 offen offset:3072 sc1 lds\n"
   "v_mfma_f32_16x16x32_bf16 v[72:75], a[68:71], v[96:99], v[72:75]\n"
   "ds_read_b128 v[92:95], v9 offset:23552\n"
   "v_mfma_f32_16x16x32_bf16 v[76:79], a[60:63], v[100:103], v[76:79]\n"
   "v_mfma_f32_16x16x32_bf16 v[116:119], a[64:67], v[100:103], v[116:119]\n"
   "v_mfma_f32_16x16x32_bf16 v[120:123], a[68:71], v[100:103], v[120:123]\n"
   "s_waitcnt vmcnt(10)\n"
   "s_barrier\n"
   "s_waitcnt lgkmcnt(2)\n"
   "v_mfma_f32_16x16x32_bf16 v[64:67], a[72:75], v[80:83], v[64:67]\n"
   "s_add_u32 m0, s90, 0x19000\n"
   "ds_read_b128 v[96:99], v9 offset:8192\n"
   "s_waitcnt vmcnt(9)\n"
   "v_xor_b32_e64 v10, s48, v17\n"
   "v_mfma_f32_16x16x32_bf16 v[68:71], a[76:79], v[80:83], v[68:71]\n"
   "s_add_u32 s58, s91, 0x28000\n"
   "v_bitop3_b32 v10, v19, s48, v10 bitop3:0xbe\n"
   "s_waitcnt vmcnt(8)\n"
   "v_bitop3_b32 v10, v21, s48, v10 bitop3:0xbe\n"
   "v_mfma_f32_16x16x32_bf16 v[72:75], a[80:83], v[80:83], v[72:75]\n"
   "ds_read_b128 v[100:103], v9 offset:24576\n"
   "v_bitop3_b32 v10, v23, s48, v10 bitop3:0xbe\n"
   "v_mfma_f32_16x16x32_bf16 v[76:79], a[72:75], v[84:87], v[76:79]\n"
   "v_cmp_ne_u32 vcc, 0, v10\n"
   "v_mfma_f32_16x16x32_bf16 v[116:119], a[76:79], v[84:87], v[116:119]\n"
   "buffer_load_dwordx4 v11, s[28:31], s58 offen lds\n"
   "v_mfma_f32_16x16x32_bf16 v[120:123], a[80:83], v[84:87], v[120:123]\n"
   "s_cbranch_vccnz .Lfb_stub32\n"
   ".Lfb_back33:\n"
   "v_add_f32 v16, v16, v20\n"
   "s_waitcnt lgkmcnt(2)\n"
   "v_mfma_f32_16x16x32_bf16 v[64:67], a[84:87], v[88:91], v[64:67]\n"
   "ds_read_b128 v[80:83], v9 offset:9216\n"
   "v_add_f32_e64 v18, v18, v22\n"
   "v_mfma_f32_16x16x32_bf16 v[68:71], a[88:91], v[88:91], v[68:71]\n"
   "v_mov_b32 v17, v16\n"
   "v_mov_b32 v19, v18\n"
   "v_mfma_f32_16x16x32_bf16 v[72:75], a[92:95], v[88:91], v[72:75]\n"
   "buffer_load_dwordx4 v12, s[28:31], s58 offen offset:1024 lds\n"
   "v_mfma_f32_16x16x32_bf16 v[76:79], a[84:87], v[92:95], v[76:79]\n"
   "ds_read_b128 v[84:87], v9 offset:25600\n"
   "v_mfma_f32_16x16x32_bf16 v[116:119], a[88:91], v[92:95], v[116:119]\n"
   "v_permlane16_swap_b32 v16, v17\n"
   "v_mfma_f32_16x16x32_bf16 v[120:123], a[92:95], v[92:95], v[120:123]\n"
   "s_waitcnt lgkmcnt(2)\n"
   "v_permlane16_swap_b32 v18, v19\n"
   "v_mfma_f32_16x16x32_bf16 v[64:67], a[96:99], v[96:99], v[64:67]\n"
   "v_add_f32 v16, v16, v17\n"
   "v_add_f32_e64 v18, v18, v19\n"
   "v_mfma_f32_16x16x32_bf16 v[68:71], a[100:103], v[96:99], v[68:71]\n"
   "buffer_load_dwordx4 v13, s[28:31], s58 offen offset:2048 lds\n"
   "v_mfma_f32_16x16x32_bf16 v[72:75], a[104:107], v[96:99], v[72:75]\n"
   "ds_read_b128 v[88:91], v9 offset:10240\n"
   "v_mov_b32_e64 v17, v16\n"
   "v_mfma_f32_16x16x32_bf16 v[76:79], a[96:99], v[100:103], v[76:79]\n"
   "ds_read_b128 v[92:95], v9 offset:26624\n"
   "v_mov_b32 v19, v18\n"
   "v_mfma_f32_16x16x32_bf16 v[116:119], a[100:103], v[100:103], v[116:119]\n"
   "v_permlane32_swap_b32 v16, v17\n"
   "v_mfma_f32_16x16x32_bf16 v[120:123], a[104:107], v[100:103], v[120:123]\n"
   "s_waitcnt lgkmcnt(2)\n"
   "v_permlane32_swap_b32 v18, v19\n"
   "v_mfma_f32_16x16x32_bf16 v[64:67], a[108:111], v[80:83], v[64:67]\n"
   "buffer_load_dwordx4 v174, s[28:31], s58 offen offset:3072 lds\n"
   "v_mfma_f32_16x16x32_bf16 v[68:71], a[112:115], v[80:83], v[68:71]\n"
   "s_add_u32 m0, s90, 0x1a000\n"
   "v_add_f32 v16, v16, v17\n"
   "v_add_f32 v18, v18, v19\n"
   "v_mfma_f32_16x16x32_bf16 v[72:75], a[116:119], v[80:83], v[72:75]\n"
   "s_add_u32 s58, s91, 0x30000\n"
   "ds_read_b128 v[96:99], v9 offset:11264\n"
   "v_mul_f32 v1, 0xbb000000, v16\n"
   "v_mfma_f32_16x16x32_bf16 v[76:79], a[108:111], v[84:87], v[76:79]\n"
   "ds_read_b128 v[100:103], v9 offset:27648\n"
   "v_mul_f32 v18, 0x3b000000, v18\n"
   "v_fma_f32 v18, -v1, v1, v18\n"
   "v_mfma_f32_16x16x32_bf16 v[116:119], a[112:115], v[84:87], v[116:119]\n"
   "v_max_f32_e64 v18, 0, v18\n"
   "v_add_f32 v18, 0x3727c5ac, v18\n"
   "v_mfma_f32_16x16x32_bf16 v[120:123], a[116:119], v[84:87], v[120:123]\n"
   "s_waitcnt lgkmcnt(2)\n"
   "v_rsq_f32 v20, v18\n"
   "v_mfma_f32_16x16x32_bf16 v[64:67], a[120:123], v[88:91], v[64:67]\n"
   "buffer_load_dwordx4 v11, s[28:31], s58 offen lds\n"
   "v_mov_b32 v21, v1\n"
   "v_mfma_f32_16x16x32_bf16 v[68:71], a[124:127], v[88:91], v[68:71]\n"
   "s_lshl_b32 s56, s51, 7\n"
   "s_add_u32 s56, s56, 0x10900\n"
   "ds_read_b128 v[80:83], v9 offset:12288\n"
   "v_mfma_f32_16x16x32_bf16 v[72:75], a[128:131], v[88:91], v[72:75]\n"
   "ds_read_b128 v[84:87], v9 offset:28672\n"
   "v_lshl_add_u32 v2, v7, 3, s56\n"
   "ds_write_b64 v2, v[20:21]\n"
   "v_mfma_f32_16x16x32_bf16 v[76:79], a[120:123], v[92:95], v[76:79]\n"
   "buffer_load_dwordx4 v12, s[28:31], s58 offen offset:1024 lds\n"
   "v_mfma_f32_16x16x32_bf16 v[116:119], a[124:127], v[92:95], v[116:119]\n"
   "v_mfma_f32_16x16x32_bf16 v[120:123], a[128:131], v[92:95], v[120:123]\n"
   "s_waitcnt vmcnt(6)\n"
   "s_waitcnt lgkmcnt(0)\n"
   "s_barrier\n"
   "v_mfma_f32_16x16x32_bf16 v[64:67], a[132:135], v[96:99], v[64:67]\n"
   "ds_read_b128 v[88:91], v9 offset:13312\n"
   "v_lshlrev_b32_e64 v2, 3, v7\n"
   "v_mfma_f32_16x16x32_bf16 v[68:71], a[136:139], v[96:99], v[68:71]\n"
   "buffer_load_dwordx4 v13, s[28:31], s58 offen offset:2048 lds\n"
   "v_mfma_f32_16x16x32_bf16 v[72:75], a[140:143], v[96:99], v[72:75]\n"
   "ds_read_b128 v[92:95], v9 offset:29696\n"
   "v_add_u32 v2, 0x10900, v2\n"
   "v_mfma_f32_16x16x32_bf16 v[76:79], a[132:135], v[100:103], v[76:79]\n"
   "v_mfma_f32_16x16x32_bf16 v[116:119], a[136:139], v[100:103], v[116:119]\n"
   "buffer_load_dwordx4 v174, s[28:31], s58 offen offset:3072 lds\n"
   "v_mfma_f32_16x16x32_bf16 v[120:123], a[140:143], v[100:103], v[120:123]\n"
   "ds_read_b64 v[16:17], v2\n"
   "v_mfma_f32_16x16x32_bf16 v[64:67], a[144:147], v[80:83], v[64:67]\n"
   "ds_read_b128 v[96:99], v9 offset:14336\n"
   "v_mfma_f32_16x16x32_bf16 v[68:71], a[148:151], v[80:83], v[68:71]\n"
   "s_add_u32 m0, s90, 0x1b000\n"
   "ds_read_b64 v[18:19], v2 offset:128\n"
   "v_mfma_f32_16x16x32_bf16 v[72:75], a[152:155], v[80:83], v[72:75]\n"
   "s_add_u32 s58, s91, 0x38000\n"
   "ds_read_b128 v[100:103], v9 offset:30720\n"
   "v_mfma_f32_16x16x32_bf16 v[76:79], a[144:147], v[84:87], v[76:79]\n"
   "ds_read_b64 v[20:21], v2 offset:256\n"
   "v_mfma_f32_16x16x32_bf16 v[116:119], a[148:151], v[84:87], v[116:119]\n"
   "v_mfma_f32_16x16x32_bf16 v[120:123], a[152:155], v[84:87], v[120:123]\n"
   "s_waitcnt lgkmcnt(5)\n"
   "ds_read_b64 v[22:23], v2 offset:384\n"
   "v_mfma_f32_16x16x32_bf16 v[64:67], a[156:159], v[88:91], v[64:67]\n"
   "ds_read_b128 v[80:83], v9 offset:15360\n"
   "v_mfma_f32_16x16x32_bf16 v[68:71], a[160:163], v[88:91], v[68:71]\n"
   "buffer_load_dwordx4 v11, s[28:31], s58 offen lds\n"
   "v_mfma_f32_16x16x32_bf16 v[72:75], a[164:167], v[88:91], v[72:75]\n"
   "ds_read_b128 v[84:87], v9 offset:31744\n"
   "v_mfma_f32_16x16x32_bf16 v[76:79], a[156:159], v[92:95], v[76:79]\n"
   "v_mfma_f32_16x16x32_bf16 v[116:119], a[160:163], v[92:95], v[116:119]\n"
   "v_mfma_f32_16x16x32_bf16 v[120:123], a[164:167], v[92:95], v[120:123]\n"
   "s_waitcnt lgkmcnt(4)\n"
   "v_mfma_f32_16x16x32_bf16 v[64:67], a[168:171], v[96:99], v[64:67]\n"
   "buffer_load_dwordx4 v12, s[28:31], s58 offen offset:1024 lds\n"
   "v_mfma_f32_16x16x32_bf16 v[68:71], a[172:175], v[96:99], v[68:71]\n"
   "ds_read_b128 v[88:91], v9 offset:32768\n"
   "v_mfma_f32_16x16x32_bf16 v[72:75], a[176:179], v[96:99], v[72:75]\n"
   "ds_read_b128 v[92:95], v9 offset:49152\n"
   "v_mfma_f32_16x16x32_bf16 v[76:79], a[168:171], v[100:103], v[76:79]\n"
   "buffer_load_dwordx4 v13, s[28:31], s58 offen offset:2048 lds\n"
   "v_mfma_f32_16x16x32_bf16 v[116:119], a[172:175], v[100:103], v[116:119]\n"
   "v_mfma_f32_16x16x32_bf16 v[120:123], a[176:179], v[100:103], v[120:123]\n"
   "s_waitcnt lgkmcnt(2)\n"
   "v_mfma_f32_16x16x32_bf16 v[64:67], a[180:183], v[80:83], v[64:67]\n"
   "ds_read_b128 v[96:99], v9 offset:33792\n"
   "v_mfma_f32_16x16x32_bf16 v[68:71], a[184:187], v[80:83], v[68:71]\n"
   "buffer_load_dwordx4 v174, s[28:31], s58 offen offset:3072 lds\n"
   "v_mfma_f32_16x16x32_bf16 v[72:75], a[188:191], v[80:83], v[72:75]\n"
   "ds_read_b128 v[100:103], v9 offset:50176\n"
   "v_mfma_f32_16x16x32_bf16 v[76:79], a[180:183], v[84:87], v[76:79]\n"
   "v_mfma_f32_16x16x32_bf16 v[116:119], a[184:187], v[84:87], v[116:119]\n"
   "v_mfma_f32_16x16x32_bf16 v[120:123], a[188:191], v[84:87], v[120:123]\n"
   "v_pk_fma_f32 v[64:65], v[36:37], v[16:17], v[64:65] op_sel:[0,1,0] op_sel_hi:[1,1,1]\n"
   "v_pk_fma_f32 v[66:67], v[38:39], v[16:17], v[66:67] op_sel:[0,1,0] op_sel_hi:[1,1,1]\n"
   "v_pk_fma_f32 v[64:65], v[64:65], v[16:17], v[232:233] op_sel_hi:[1,0,1]\n"
   "v_pk_fma_f32 v[66:67], v[66:67], v[16:17], v[234:235] op_sel_hi:[1,0,1]\n"
   "v_cvt_pk_bf16_f32 v192, v64, v65\n"
   "v_cvt_pk_bf16_f32 v193, v66, v67\n"
   "v_pk_fma_f32 v[76:77], v[36:37], v[18:19], v[76:77] op_sel:[0,1,0] op_sel_hi:[1,1,1]\n"
   "v_pk_fma_f32 v[78:79], v[38:39], v[18:19], v[78:79] op_sel:[0,1,0] op_sel_hi:[1,1,1]\n"
   "v_pk_fma_f32 v[76:77], v[76:77], v[18:19], v[232:233] op_sel_hi:[1,0,1]\n"
   "v_pk_fma_f32 v[78:79], v[78:79], v[18:19], v[234:235] op_sel_hi:[1,0,1]\n"
   "v_cvt_pk_bf16_f32 v194, v76, v77\n"
   "v_cvt_pk_bf16_f32 v195, v78, v79\n"
   "v_pk_max_i16 v192, v192, 0\n"
   "v_pk_max_i16 v194, v194, 0\n"
   "v_pk_max_i16 v193, v193, 0\n"
   "v_pk_max_i16 v195, v195, 0\n"
   "v_permlane32_swap_b32 v192, v194\n"
   "s_nop 0\n"
   "v_permlane32_swap_b32 v193, v195\n"
   "v_or_b32 v192, v14, v192\n"
   "v_or_b32 v194, v114, v194\n"
   "v_or_b32 v193, v15, v193\n"
   "v_or_b32_e64 v195, v115, v195\n"
   "buffer_store_dwordx4 v[192:195], v161, s[44:47], s82 offen sc1\n"
   "v_pk_fma_f32 v[68:69], v[40:41], v[16:17], v[68:69] op_sel:[0,1,0] op_sel_hi:[1,1,1]\n"
   "v_pk_fma_f32 v[70:71], v[42:43], v[16:17], v[70:71] op_sel:[0,1,0] op_sel_hi:[1,1,1]\n"
   "v_pk_fma_f32 v[68:69], v[68:69], v[16:17], v[236:237] op_sel_hi:[1,0,1]\n"
   "v_pk_fma_f32 v[70:71], v[70:71], v[16:17], v[238:239] op_sel_hi:[1,0,1]\n"
   "v_cvt_pk_bf16_f32 v196, v68, v69\n"
   "v_cvt_pk_bf16_f32 v197, v70, v71\n"
   "v_pk_fma_f32 v[116:117], v[40:41], v[18:19], v[116:117] op_sel:[0,1,0] op_sel_hi:[1,1,1]\n"
   "v_pk_fma_f32 v[118:119], v[42:43], v[18:19], v[118:119] op_sel:[0,1,0] op_sel_hi:[1,1,1]\n"
   "v_pk_fma_f32 v[116:117], v[116:117], v[18:19], v[236:237] op_sel_hi:[1,0,1]\n"
   "v_pk_fma_f32 v[118:119], v[118:119], v[18:19], v[238:239] op_sel_hi:[1,0,1]\n"
   "v_cvt_pk_bf16_f32 v198, v116, v117\n"
   "v_cvt_pk_bf16_f32 v199, v118, v119\n"
   "v_pk_max_i16 v196, v196, 0\n"
   "v_pk_max_i16 v198, v198, 0\n"
   "v_pk_max_i16 v197, v197, 0\n"
   "v_pk_max_i16 v199, v199, 0\n"
   "v_permlane32_swap_b32 v196, v198\n"
   "s_nop 0\n"
   "v_permlane32_swap_b32 v197, v199\n"
   "v_or_b32 v196, v14, v196\n"
   "v_or_b32 v198, v114, v198\n"
   "v_or_b32 v197, v15, v197\n"
   "v_or_b32_e64 v199, v115, v199\n"
   "buffer_store_dwordx4 v[196:199], v161, s[44:47], s87 offen sc1\n"
   "v_pk_fma_f32 v[72:73], v[44:45], v[16:17], v[72:73] op_sel:[0,1,0] op_sel_hi:[1,1,1]\n"
   "v_pk_fma_f32 v[74:75], v[46:47], v[16:17], v[74:75] op_sel:[0,1,0] op_sel_hi:[1,1,1]\n"
   "v_pk_fma_f32 v[72:73], v[72:73], v[16:17], v[240:241] op_sel_hi:[1,0,1]\n"
   "v_pk_fma_f32 v[74:75], v[74:75], v[16:17], v[242:243] op_sel_hi:[1,0,1]\n"
   "v_cvt_pk_bf16_f32 v216, v72, v73\n"
   "v_cvt_pk_bf16_f32 v217, v74, v75\n"
   "v_pk_fma_f32 v[120:121], v[44:45], v[18:19], v[120:121] op_sel:[0,1,0] op_sel_hi:[1,1,1]\n"
   "v_pk_fma_f32 v[122:123], v[46:47], v[18:19], v[122:123] op_sel:[0,1,0] op_sel_hi:[1,1,1]\n"
   "v_pk_fma_f32 v[120:121], v[120:121], v[18:19], v[240:241] op_sel_hi:[1,0,1]\n"
   "v_pk_fma_f32 v[122:123], v[122:123], v[18:19], v[242:243] op_sel_hi:[1,0,1]\n"
   "v_cvt_pk_bf16_f32 v218, v120, v121\n"
   "v_cvt_pk_bf16_f32 v219, v122, v123\n"
   "v_pk_max_i16 v216, v216, 0\n"
   "v_pk_max_i16 v218, v218, 0\n"
   "v_pk_max_i16 v217, v217, 0\n"
   "v_pk_max_i16 v219, v219, 0\n"
   "v_permlane32_swap_b32 v216, v218\n"
   "s_nop 0\n"
   "v_permlane32_swap_b32 v217, v219\n"
   "v_or_b32 v216, v14, v216\n"
   "v_or_b32 v218, v114, v218\n"
   "v_or_b32 v217, v15, v217\n"
   "v_or_b32 v219, v115, v219\n"
   "buffer_store_dwordx4 v[216:219], v161, s[44:47], s73 offen sc1\n"
   "s_waitcnt lgkmcnt(2)\n"
   "v_mfma_f32_16x16x32_bf16 v[64:67], a[0:3], v[88:91], 0\n"
   "s_mul_i32 s56, s51, 0x2c00\n"
   "ds_read_b128 v[80:83], v9 offset:34816\n"
   "v_mfma_f32_16x16x32_bf16 v[68:71], a[4:7], v[88:91], 0\n"
   "s_add_u32 s71, s71, s56\n"
   "v_mfma_f32_16x16x32_bf16 v[72:75], a[8:11], v[88:91], 0\n"
   "s_mul_i32 s56, s66, 0xac00\n"
   "ds_read_b128 v[84:87], v9 offset:51200\n"
   "v_mfma_f32_16x16x32_bf16 v[76:79], a[0:3], v[92:95], 0\n"
   "v_mfma_f32_16x16x32_bf16 v[116:119], a[4:7], v[92:95], 0\n"
   "s_add_u32 s17, s71, s56\n"
   "v_mfma_f32_16x16x32_bf16 v[120:123], a[8:11], v[92:95], 0\n"
   "s_waitcnt lgkmcnt(2)\n"
   "s_add_u32 s19, s17, 0x15800\n"
   "v_mfma_f32_16x16x32_bf16 v[64:67], a[12:15], v[96:99], v[64:67]\n"
   "ds_read_b128 v[88:91], v9 offset:35840\n"
   "v_mfma_f32_16x16x32_bf16 v[68:71], a[16:19], v[96:99], v[68:71]\n"
   "s_sub_u32 s56, 0xac00, s56\n"
   "v_mfma_f32_16x16x32_bf16 v[72:75], a[20:23], v[96:99], v[72:75]\n"
   "s_add_u32 s16, s71, s56\n"
   "ds_read_b128 v[92:95], v9 offset:52224\n"
   "v_mfma_f32_16x16x32_bf16 v[76:79], a[12:15], v[100:103], v[76:79]\n"
   "s_add_u32 s18, s16, 0x15800\n"
   "v_mfma_f32_16x16x32_bf16 v[116:119], a[16:19], v[100:103], v[116:119]\n"
   "v_mfma_f32_16x16x32_bf16 v[120:123], a[20:23], v[100:103], v[120:123]\n"
   "s_waitcnt lgkmcnt(2)\n"
   "s_cmp_eq_u32 s51, 3\n"
   "s_cselect_b32 s56, 7, 8\n"
   "v_mfma_f32_16x16x32_bf16 v[64:67], a[24:27], v[80:83], v[64:67]\n"
   "s_lshl_b32 s56, s56, 10\n"
   "ds_read_b128 v[96:99], v9 offset:36864\n"
   "v_mfma_f32_16x16x32_bf16 v[68:71], a[28:31], v[80:83], v[68:71]\n"
   "ds_read_b64_tr_b16 a[0:1], v172\n"
   "ds_read_b64_tr_b16 a[2:3], v172 offset:1024\n"
   "v_mfma_f32_16x16x32_bf16 v[72:75], a[32:35], v[80:83], v[72:75]\n"
   "ds_read_b128 v[100:103], v9 offset:53248\n"
   "ds_read_b64_tr_b16 a[4:5], v173\n"
   "v_mfma_f32_16x16x32_bf16 v[76:79], a[24:27], v[84:87], v[76:79]\n"
   "ds_read_b64_tr_b16 a[6:7], v173 offset:1024\n"
   "v_add_u32 v6, 0x1000, v9\n"
   "v_mfma_f32_16x16x32_bf16 v[116:119], a[28:31], v[84:87], v[116:119]\n"
   "ds_read_b64_tr_b16 a[8:9], v175\n"
   "v_add_u32 v162, 0x2000, v9\n"
   "v_mfma_f32_16x16x32_bf16 v[120:123], a[32:35], v[84:87], v[120:123]\n"
   "s_waitcnt lgkmcnt(7)\n"
   "ds_read_b64_tr_b16 a[10:11], v175 offset:1024\n"
   "v_add_u32 v163, s56, v9\n"
   "v_mfma_f32_16x16x32_bf16 v[64:67], a[36:39], v[88:91], v[64:67]\n"
   "ds_read_b128 v[80:83], v9 offset:37888\n"
   "ds_read_b64_tr_b16 a[12:13], v176\n"
   "v_mfma_f32_16x16x32_bf16 v[68:71], a[40:43], v[88:91], v[68:71]\n"
   "s_add_u32 m0, s90, 0x18000\n"
   "ds_read_b64_tr_b16 a[14:15], v176 offset:1024\n"
   "v_lshlrev_b32_e64 v124, 16, v32\n"
   "v_mfma_f32_16x16x32_bf16 v[72:75], a[44:47], v[88:91], v[72:75]\n"
   "ds_read_b128 v[84:87], v9 offset:54272\n"
   "v_and_b32 v125, s65, v32\n"
   "v_mfma_f32_16x16x32_bf16 v[76:79], a[36:39], v[92:95], v[76:79]\n"
   "s_waitcnt vmcnt(11)\n"
   "ds_read_b64_tr_b16 a[16:17], v172 offset:4096\n"
   "ds_read_b64_tr_b16 a[18:19], v172 offset:5120\n"
   "v_mfma_f32_16x16x32_bf16 v[116:119], a[40:43], v[92:95], v[116:119]\n"
   "s_add_u32 s58, s91, 0x40000\n"
   "ds_read_b64_tr_b16 a[20:21], v173 offset:4096\n"
   "v_lshlrev_b32 v126, 16, v33\n"
   "v_mfma_f32_16x16x32_bf16 v[120:123], a[44:47], v[92:95], v[120:123]\n"
   "s_waitcnt lgkmcnt(11)\n"
   "ds_read_b64_tr_b16 a[22:23], v173 offset:5120\n"
   "v_and_b32 v127, s65, v33\n"
   "v_mfma_f32_16x16x32_bf16 v[64:67], a[48:51], v[96:99], v[64:67]\n"
   "s_waitcnt lgkmcnt(5)\n"
   "buffer_load_dwordx4 v11, s[28:31], s58 offen lds\n"
   "ds_read_b64_tr_b16 a[24:25], v175 offset:4096\n"
   "v_mfma_f32_16x16x32_bf16 v[68:71], a[52:55], v[96:99], v[68:71]\n"
   "ds_read_b128 v[88:91], v9 offset:38912\n"
   "ds_read_b64_tr_b16 a[26:27], v175 offset:5120\n"
   "v_lshlrev_b32_e64 v128, 16, v34\n"
   "v_mfma_f32_16x16x32_bf16 v[72:75], a[56:59], v[96:99], v[72:75]\n"
   "ds_read_b128 v[92:95], v9 offset:55296\n"
   "ds_read_b64_tr_b16 a[28:29], v176 offset:4096\n"
   "v_and_b32_e64 v129, s65, v34\n"
   "v_mfma_f32_16x16x32_bf16 v[76:79], a[48:51], v[100:103], v[76:79]\n"
   "ds_read_b64_tr_b16 a[30:31], v176 offset:5120\n"
   "buffer_load_dwordx4 v12, s[28:31], s58 offen offset:1024 lds\n"
   "v_mfma_f32_16x16x32_bf16 v[116:119], a[52:55], v[100:103], v[116:119]\n"
   "s_waitcnt vmcnt(9)\n"
   "ds_read_b64_tr_b16 a[32:33], v172 offset:8192\n"
   "v_lshlrev_b32_e64 v130, 16, v35\n"
   "v_mfma_f32_16x16x32_bf16 v[120:123], a[56:59], v[100:103], v[120:123]\n"
   "s_waitcnt lgkmcnt(11)\n"
   "ds_read_b64_tr_b16 a[34:35], v172 offset:9216\n"
   "v_and_b32 v131, s65, v35\n"
   "v_mfma_f32_16x16x32_bf16 v[64:67], a[60:63], v[80:83], v[64:67]\n"
   "ds_read_b64_tr_b16 a[36:37], v173 offset:8192\n"
   "buffer_load_dwordx4 v13, s[28:31], s58 offen offset:2048 lds\n"
   "v_mfma_f32_16x16x32_bf16 v[68:71], a[64:67], v[80:83], v[68:71]\n"
   "s_lshl3_add_u32 s56, s66, s12\n"
   "ds_read_b128 v[96:99], v9 offset:39936\n"
   "ds_read_b64_tr_b16 a[38:39], v173 offset:9216\n"
   "v_mfma_f32_16x16x32_bf16 v[72:75], a[68:71], v[80:83], v[72:75]\n"
   "ds_read_b64_tr_b16 a[40:41], v175 offset:8192\n"
   "buffer_load_dwordx4 v174, s[28:31], s58 offen offset:3072 lds\n"
   "v_mfma_f32_16x16x32_bf16 v[76:79], a[60:63], v[84:87], v[76:79]\n"
   "s_lshl_b32 s56, s56, 11\n"
   "s_add_u32 s57, s56, s61\n"
   "ds_read_b128 v[100:103], v9 offset:56320\n"
   "v_mfma_f32_16x16x32_bf16 v[116:119], a[64:67], v[84:87], v[116:119]\n"
   "s_add_u32 m0, s90, 0x19000\n"
   "ds_read_b64_tr_b16 a[42:43], v175 offset:9216\n"
   "ds_read_b64_tr_b16 a[44:45], v176 offset:8192\n"
   "v_mfma_f32_16x16x32_bf16 v[120:123], a[68:71], v[84:87], v[120:123]\n"
   "s_waitcnt lgkmcnt(11)\n"
   "ds_read_b64_tr_b16 a[46:47], v176 offset:9216\n"
   "v_add_u32 v1, s57, v9\n"
   "v_mfma_f32_16x16x32_bf16 v[64:67], a[72:75], v[88:91], v[64:67]\n"
   "s_add_u32 s58, s91, 0x48000\n"
   "ds_read_b128 v[80:83], v9 offset:40960\n"
   "v_xor_b32 v164, 0x4000, v1\n"
   "v_mfma_f32_16x16x32_bf16 v[68:71], a[76:79], v[88:91], v[68:71]\n"
   "s_waitcnt lgkmcnt(11)\n"
   "buffer_load_dwordx4 v11, s[28:31], s58 offen lds\n"
   "v_mfma_f32_16x16x32_bf16 v[72:75], a[80:83], v[88:91], v[72:75]\n"
   "ds_read_b128 v[84:87], v9 offset:57344\n"
   "v_xor_b32 v165, 0x4800, v1\n"
   "v_mfma_f32_16x16x32_bf16 v[76:79], a[72:75], v[92:95], v[76:79]\n"
   "s_waitcnt vmcnt(8)\n"
   "ds_read_b64_tr_b16 a[48:49], v172 offset:12288\n"
   "v_xor_b32 v180, 0x800, v1\n"
   "v_mfma_f32_16x16x32_bf16 v[116:119], a[76:79], v[92:95], v[116:119]\n"
   "buffer_load_dwordx4 v12, s[28:31], s58 offen offset:1024 lds\n"
   "v_mfma_f32_16x16x32_bf16 v[120:123], a[80:83], v[92:95], v[120:123]\n"
   "s_waitcnt lgkmcnt(6)\n"
   "ds_read_b64_tr_b16 a[50:51], v172 offset:13312\n"
   "v_xor_b32 v177, 0x2000, v1\n"
   "v_mfma_f32_16x16x32_bf16 v[64:67], a[84:87], v[96:99], v[64:67]\n"
   "buffer_load_dwordx4 v13, s[28:31], s58 offen offset:2048 lds\n"
   "v_mfma_f32_16x16x32_bf16 v[68:71], a[88:91], v[96:99], v[68:71]\n"
   "s_xor_b32 s57, s66, 1\n"
   "ds_read_b64_tr_b16 a[52:53], v173 offset:12288\n"
   "buffer_load_dwordx4 v174, s[28:31], s58 offen offset:3072 lds\n"
   "v_mfma_f32_16x16x32_bf16 v[72:75], a[92:95], v[96:99], v[72:75]\n"
   "s_lshl_b32 s57, s57, 12\n"
   "s_add_u32 s57, s57, s56\n"
   "ds_read_b128 v[88:91], v9 offset:41984\n"
   "v_mfma_f32_16x16x32_bf16 v[76:79], a[84:87], v[100:103], v[76:79]\n"
   "ds_read_b64_tr_b16 a[54:55], v173 offset:13312\n"
   "ds_read_b128 v[92:95], v9 offset:58368\n"
   "v_mfma_f32_16x16x32_bf16 v[116:119], a[88:91], v[100:103], v[116:119]\n"
   "s_add_u32 m0, s90, 0x1a000\n"
   "ds_read_b64_tr_b16 a[56:57], v175 offset:12288\n"
   "v_add_u32_e64 v181, s57, v9\n"
   "v_mfma_f32_16x16x32_bf16 v[120:123], a[92:95], v[100:103], v[120:123]\n"
   "s_waitcnt lgkmcnt(7)\n"
   "s_add_u32 s58, s91, 0x50000\n"
   "ds_read_b64_tr_b16 a[58:59], v175 offset:13312\n"
   "v_mfma_f32_16x16x32_bf16 v[64:67], a[96:99], v[80:83], v[64:67]\n"
   "s_lshl_b32 s56, s66, 14\n"
   "s_lshl_b32 s57, s12, 6\n"
   "ds_read_b64_tr_b16 a[60:61], v176 offset:12288\n"
   "v_mfma_f32_16x16x32_bf16 v[68:71], a[100:103], v[80:83], v[68:71]\n"
   "buffer_load_dwordx4 v11, s[28:31], s58 offen lds\n"
   "ds_read_b64_tr_b16 a[62:63], v176 offset:13312\n"
   "v_mfma_f32_16x16x32_bf16 v[72:75], a[104:107], v[80:83], v[72:75]\n"
   "s_add_u32 s56, s56, s57\n"
   "ds_read_b128 v[96:99], v9 offset:43008\n"
   "v_mfma_f32_16x16x32_bf16 v[76:79], a[96:99], v[84:87], v[76:79]\n"
   "ds_read_b128 v[100:103], v9 offset:59392\n"
   "buffer_load_dwordx4 v12, s[28:31], s58 offen offset:1024 lds\n"
   "v_mfma_f32_16x16x32_bf16 v[116:119], a[100:103], v[84:87], v[116:119]\n"
   "buffer_load_dwordx4 v13, s[28:31], s58 offen offset:2048 lds\n"
   "v_mfma_f32_16x16x32_bf16 v[120:123], a[104:107], v[84:87], v[120:123]\n"
   "s_waitcnt lgkmcnt(6)\n"
   "s_add_u32 s56, s56, s70\n"
   "s_add_u32 s32, s56, s79\n"
   "buffer_load_dwordx4 v174, s[28:31], s58 offen offset:3072 lds\n"
   "v_mfma_f32_16x16x32_bf16 v[64:67], a[108:111], v[88:91], v[64:67]\n"
   "ds_read_b128 v[80:83], v9 offset:44032\n"
   "v_mfma_f32_16x16x32_bf16 v[68:71], a[112:115], v[88:91], v[68:71]\n"
   "v_mfma_f32_16x16x32_bf16 v[72:75], a[116:119], v[88:91], v[72:75]\n"
   "ds_read_b128 v[84:87], v9 offset:60416\n"
   "v_mfma_f32_16x16x32_bf16 v[76:79], a[108:111], v[92:95], v[76:79]\n"
   "v_mfma_f32_16x16x32_bf16 v[116:119], a[112:115], v[92:95], v[116:119]\n"
   "v_mfma_f32_16x16x32_bf16 v[120:123], a[116:119], v[92:95], v[120:123]\n"
   "s_waitcnt lgkmcnt(2)\n"
   "v_mfma_f32_16x16x32_bf16 v[64:67], a[120:123], v[96:99], v[64:67]\n"
   "ds_read_b128 v[88:91], v9 offset:45056\n"
   "v_mfma_f32_16x16x32_bf16 v[68:71], a[124:127], v[96:99], v[68:71]\n"
   "s_waitcnt vmcnt(8)\n"
   "ds_read_b64_tr_b16 a[64:65], v172\n"
   "v_mfma_f32_16x16x32_bf16 v[72:75], a[128:131], v[96:99], v[72:75]\n"
   "ds_read_b64_tr_b16 a[66:67], v172 offset:1024\n"
   "v_mfma_f32_16x16x32_bf16 v[76:79], a[120:123], v[100:103], v[76:79]\n"
   "ds_read_b128 v[92:95], v9 offset:61440\n"
   "v_mfma_f32_16x16x32_bf16 v[116:119], a[124:127], v[100:103], v[116:119]\n"
   "ds_read_b64_tr_b16 a[68:69], v173\n"
   "buffer_load_dwordx4 v[132:135], v9, s[44:47], s16 offen sc1\n"
   "v_mfma_f32_16x16x32_bf16 v[120:123], a[128:131], v[100:103], v[120:123]\n"
   "s_waitcnt lgkmcnt(5)\n"
   "ds_read_b64_tr_b16 a[70:71], v173 offset:1024\n"
   "v_mfma_f32_16x16x32_bf16 v[64:67], a[132:135], v[80:83], v[64:67]\n"
   "ds_read_b64_tr_b16 a[72:73], v175\n"
   "v_mfma_f32_16x16x32_bf16 v[68:71], a[136:139], v[80:83], v[68:71]\n"
   "ds_read_b128 v[96:99], v9 offset:46080\n"
   "ds_read_b64_tr_b16 a[74:75], v175 offset:1024\n"
   "v_mfma_f32_16x16x32_bf16 v[72:75], a[140:143], v[80:83], v[72:75]\n"
   "ds_read_b64_tr_b16 a[76:77], v176\n"
   "buffer_load_dwordx4 v[136:139], v9, s[44:47], s16 offen offset:1024 sc1\n"
   "v_mfma_f32_16x16x32_bf16 v[76:79], a[132:135], v[84:87], v[76:79]\n"
   "ds_read_b128 v[100:103], v9 offset:62464\n"
   "ds_read_b64_tr_b16 a[78:79], v176 offset:1024\n"
   "v_mfma_f32_16x16x32_bf16 v[116:119], a[136:139], v[84:87], v[116:119]\n"
   "buffer_load_dwordx4 v[140:143], v9, s[44:47], s16 offen offset:2048 sc1\n"
   "v_mfma_f32_16x16x32_bf16 v[120:123], a[140:143], v[84:87], v[120:123]\n"
   "s_waitcnt lgkmcnt(8)\n"
   "v_mfma_f32_16x16x32_bf16 v[64:67], a[144:147], v[88:91], v[64:67]\n"
   "ds_read_b128 v[80:83], v9 offset:47104\n"
   "v_mfma_f32_16x16x32_bf16 v[68:71], a[148:151], v[88:91], v[68:71]\n"
   "s_waitcnt vmcnt(7)\n"
   "ds_read_b64_tr_b16 a[80:81], v172 offset:4096\n"
   "v_mfma_f32_16x16x32_bf16 v[72:75], a[152:155], v[88:91], v[72:75]\n"
   "ds_read_b64_tr_b16 a[82:83], v172 offset:5120\n"
   "v_mfma_f32_16x16x32_bf16 v[76:79], a[144:147], v[92:95], v[76:79]\n"
   "ds_read_b128 v[84:87], v9 offset:63488\n"
   "v_mfma_f32_16x16x32_bf16 v[116:119], a[148:151], v[92:95], v[116:119]\n"
   "ds_read_b64_tr_b16 a[84:85], v173 offset:4096\n"
   "v_mfma_f32_16x16x32_bf16 v[120:123], a[152:155], v[92:95], v[120:123]\n"
   "s_waitcnt lgkmcnt(6)\n"
   "ds_read_b64_tr_b16 a[86:87], v173 offset:5120\n"
   "v_mfma_f32_16x16x32_bf16 v[64:67], a[156:159], v[96:99], v[64:67]\n"
   "ds_read_b64_tr_b16 a[88:89], v175 offset:4096\n"
   "buffer_load_dwordx4 v[144:147], v9, s[44:47], s16 offen offset:3072 sc1\n"
   "v_mfma_f32_16x16x32_bf16 v[68:71], a[160:163], v[96:99], v[68:71]\n"
   "ds_read_b128 v[88:91], v9 offset:48128\n"
   "ds_read_b64_tr_b16 a[90:91], v175 offset:5120\n"
   "v_mfma_f32_16x16x32_bf16 v[72:75], a[164:167], v[96:99], v[72:75]\n"
   "ds_read_b64_tr_b16 a[92:93], v176 offset:4096\n"
   "v_mfma_f32_16x16x32_bf16 v[76:79], a[156:159], v[100:103], v[76:79]\n"
   "ds_read_b128 v[92:95], v9 offset:64512\n"
   "ds_read_b64_tr_b16 a[94:95], v176 offset:5120\n"
   "buffer_load_dwordx4 v[148:151], v6, s[44:47], s16 offen sc1\n"
   "v_mfma_f32_16x16x32_bf16 v[116:119], a[160:163], v[100:103], v[116:119]\n"
   "v_mfma_f32_16x16x32_bf16 v[120:123], a[164:167], v[100:103], v[120:123]\n"
   "s_waitcnt lgkmcnt(0)\n"
   "s_barrier\n"
   "v_mfma_f32_16x16x32_bf16 v[64:67], a[168:171], v[80:83], v[64:67]\n"
   "s_waitcnt vmcnt(5)\n"
   "ds_read_b64_tr_b16 a[96:97], v172 offset:8192\n"
   "v_mfma_f32_16x16x32_bf16 v[68:71], a[172:175], v[80:83], v[68:71]\n"
   "buffer_load_dwordx4 v[152:155], v6, s[44:47], s16 offen offset:1024 sc1\n"
   "v_mfma_f32_16x16x32_bf16 v[72:75], a[176:179], v[80:83], v[72:75]\n"
   "ds_read_b64_tr_b16 a[98:99], v172 offset:9216\n"
   "v_mfma_f32_16x16x32_bf16 v[76:79], a[168:171], v[84:87], v[76:79]\n"
   "ds_read_b64_tr_b16 a[100:101], v173 offset:8192\n"
   "v_mfma_f32_16x16x32_bf16 v[116:119], a[172:175], v[84:87], v[116:119]\n"
   "buffer_load_dwordx4 v[156:159], v6, s[44:47], s16 offen offset:2048 sc1\n"
   "v_mfma_f32_16x16x32_bf16 v[120:123], a[176:179], v[84:87], v[120:123]\n"
   "ds_read_b64_tr_b16 a[102:103], v173 offset:9216\n"
   "v_mfma_f32_16x16x32_bf16 v[64:67], a[180:183], v[88:91], v[64:67]\n"
   "ds_read_b64_tr_b16 a[104:105], v175 offset:8192\n"
   "v_mfma_f32_16x16x32_bf16 v[68:71], a[184:187], v[88:91], v[68:71]\n"
   "ds_read_b64_tr_b16 a[106:107], v175 offset:9216\n"
   "v_mfma_f32_16x16x32_bf16 v[72:75], a[188:191], v[88:91], v[72:75]\n"
   "buffer_load_dwordx4 v[168:171], v6, s[44:47], s16 offen offset:3072 sc1\n"
   "v_mfma_f32_16x16x32_bf16 v[76:79], a[180:183], v[92:95], v[76:79]\n"
   "ds_read_b64_tr_b16 a[108:109], v176 offset:8192\n"
   "v_mfma_f32_16x16x32_bf16 v[116:119], a[184:187], v[92:95], v[116:119]\n"
   "ds_read_b64_tr_b16 a[110:111], v176 offset:9216\n"
   "v_mfma_f32_16x16x32_bf16 v[120:123], a[188:191], v[92:95], v[120:123]\n"
   "v_pk_fma_f32 v[64:65], v[36:37], v[20:21], v[64:65] op_sel:[0,1,0] op_sel_hi:[1,1,1]\n"
   "v_pk_fma_f32 v[66:67], v[38:39], v[20:21], v[66:67] op_sel:[0,1,0] op_sel_hi:[1,1,1]\n"
   "v_pk_fma_f32 v[64:65], v[64:65], v[20:21], v[232:233] op_sel_hi:[1,0,1]\n"
   "v_pk_fma_f32 v[66:67], v[66:67], v[20:21], v[234:235] op_sel_hi:[1,0,1]\n"
   "v_cvt_pk_bf16_f32 v192, v64, v65\n"
   "v_cvt_pk_bf16_f32 v193, v66, v67\n"
   "v_pk_fma_f32 v[76:77], v[36:37], v[22:23], v[76:77] op_sel:[0,1,0] op_sel_hi:[1,1,1]\n"
   "v_pk_fma_f32 v[78:79], v[38:39], v[22:23], v[78:79] op_sel:[0,1,0] op_sel_hi:[1,1,1]\n"
   "v_pk_fma_f32 v[76:77], v[76:77], v[22:23], v[232:233] op_sel_hi:[1,0,1]\n"
   "v_pk_fma_f32 v[78:79], v[78:79], v[22:23], v[234:235] op_sel_hi:[1,0,1]\n"
   "v_cvt_pk_bf16_f32 v194, v76, v77\n"
   "v_cvt_pk_bf16_f32 v195, v78, v79\n"
   "buffer_load_dwordx4 v[184:187], v162, s[44:47], s16 offen sc1\n"
   "buffer_load_dwordx4 v[200:203], v162, s[44:47], s16 offen offset:1024 sc1\n"
   "buffer_load_dwordx4 v[204:207], v163, s[44:47], s16 offen offset:2048 sc1\n"
   "buffer_load_dwordx4 v[212:215], v9, s[44:47], s17 offen sc1\n"
   "v_pk_max_i16 v192, v192, 0\n"
   "v_pk_max_i16 v194, v194, 0\n"
   "v_pk_max_i16 v193, v193, 0\n"
   "v_pk_max_i16 v195, v195, 0\n"
   "v_permlane32_swap_b32 v192, v194\n"
   "s_nop 0\n"
   "v_permlane32_swap_b32 v193, v195\n"
   "v_or_b32 v192, v14, v192\n"
   "v_or_b32 v194, v114, v194\n"
   "v_or_b32 v193, v15, v193\n"
   "v_or_b32_e64 v195, v115, v195\n"
   "buffer_store_dwordx4 v[192:195], v161, s[44:47], s74 offen sc1\n"
   "v_pk_fma_f32 v[68:69], v[40:41], v[20:21], v[68:69] op_sel:[0,1,0] op_sel_hi:[1,1,1]\n"
   "v_pk_fma_f32 v[70:71], v[42:43], v[20:21], v[70:71] op_sel:[0,1,0] op_sel_hi:[1,1,1]\n"
   "v_pk_fma_f32 v[68:69], v[68:69], v[20:21], v[236:237] op_sel_hi:[1,0,1]\n"
   "v_pk_fma_f32 v[70:71], v[70:71], v[20:21], v[238:239] op_sel_hi:[1,0,1]\n"
   "v_cvt_pk_bf16_f32 v196, v68, v69\n"
   "v_cvt_pk_bf16_f32 v197, v70, v71\n"
   "v_pk_fma_f32 v[116:117], v[40:41], v[22:23], v[116:117] op_sel:[0,1,0] op_sel_hi:[1,1,1]\n"
   "v_pk_fma_f32 v[118:119], v[42:43], v[22:23], v[118:119] op_sel:[0,1,0] op_sel_hi:[1,1,1]\n"
   "v_pk_fma_f32 v[116:117], v[116:117], v[22:23], v[236:237] op_sel_hi:[1,0,1]\n"
   "v_pk_fma_f32 v[118:119], v[118:119], v[22:23], v[238:239] op_sel_hi:[1,0,1]\n"
   "v_cvt_pk_bf16_f32 v198, v116, v117\n"
   "v_cvt_pk_bf16_f32 v199, v118, v119\n"
   "v_pk_max_i16 v196, v196, 0\n"
   "v_pk_max_i16 v198, v198, 0\n"
   "v_pk_max_i16 v197, v197, 0\n"
   "v_pk_max_i16 v199, v199, 0\n"
   "v_permlane32_swap_b32 v196, v198\n"
   "s_nop 0\n"
   "v_permlane32_swap_b32 v197, v199\n"
   "v_or_b32 v196, v14, v196\n"
   "v_or_b32 v198, v114, v198\n"
   "v_or_b32 v197, v15, v197\n"
   "v_or_b32_e64 v199, v115, v199\n"
   "buffer_store_dwordx4 v[196:199], v161, s[44:47], s77 offen sc1\n"
   "v_pk_fma_f32 v[72:73], v[44:45], v[20:21], v[72:73] op_sel:[0,1,0] op_sel_hi:[1,1,1]\n"
   "v_pk_fma_f32 v[74:75], v[46:47], v[20:21], v[74:75] op_sel:[0,1,0] op_sel_hi:[1,1,1]\n"
   "v_pk_fma_f32 v[72:73], v[72:73], v[20:21], v[240:241] op_sel_hi:[1,0,1]\n"
   "v_pk_fma_f32 v[74:75], v[74:75], v[20:21], v[242:243] op_sel_hi:[1,0,1]\n"
   "v_cvt_pk_bf16_f32 v216, v72, v73\n"
   "v_cvt_pk_bf16_f32 v217, v74, v75\n"
   "v_pk_fma_f32 v[120:121], v[44:45], v[22:23], v[120:121] op_sel:[0,1,0] op_sel_hi:[1,1,1]\n"
   "v_pk_fma_f32 v[122:123], v[46:47], v[22:23], v[122:123] op_sel:[0,1,0] op_sel_hi:[1,1,1]\n"
   "v_pk_fma_f32 v[120:121], v[120:121], v[22:23], v[240:241] op_sel_hi:[1,0,1]\n"
   "v_pk_fma_f32 v[122:123], v[122:123], v[22:23], v[242:243] op_sel_hi:[1,0,1]\n"
   "v_cvt_pk_bf16_f32 v218, v120, v121\n"
   "v_cvt_pk_bf16_f32 v219, v122, v123\n"
   "v_pk_max_i16 v216, v216, 0\n"
   "v_pk_max_i16 v218, v218, 0\n"
   "v_pk_max_i16 v217, v217, 0\n"
   "v_pk_max_i16 v219, v219, 0\n"
   "v_permlane32_swap_b32 v216, v218\n"
   "s_nop 0\n"
   "v_permlane32_swap_b32 v217, v219\n"
   "v_or_b32 v216, v14, v216\n"
   "v_or_b32 v218, v114, v218\n"
   "v_or_b32 v217, v15, v217\n"
   "v_or_b32_e64 v219, v115, v219\n"
   "buffer_store_dwordx4 v[216:219], v161, s[44:47], s78 offen sc1\n"
   "s_waitcnt vmcnt(14)\n"
   "v_xor_b32 v132, v14, v132\n"
   "v_xor_b32 v133, v15, v133\n"
   "v_xor_b32 v134, v114, v134\n"
   "v_xor_b32_e64 v135, v115, v135\n"
   "v_or3_b32 v1, v132, v133, v133\n"
   "v_or3_b32 v1, v134, v135, v1\n"
   "v_mfma_f32_16x16x32_bf16 v[16:19], a[192:195], v[132:135], 0\n"
   "v_mfma_f32_16x16x32_bf16 v[20:23], a[196:199], v[132:135], 0\n"
   "s_waitcnt vmcnt(13)\n"
   "v_xor_b32 v136, v14, v136\n"
   "v_xor_b32_e64 v137, v15, v137\n"
   "v_mfma_f32_16x16x32_bf16 v[24:27], a[200:203], v[132:135], 0\n"
   "v_xor_b32 v138, v114, v138\n"
   "v_xor_b32 v139, v115, v139\n"
   "v_mfma_f32_16x16x32_bf16 v[28:31], a[204:207], v[132:135], 0\n"
   "v_or3_b32 v1, v136, v137, v1\n"
   "v_or3_b32 v1, v138, v139, v1\n"
   "v_mfma_f32_16x16x32_bf16 v[16:19], a[208:211], v[136:139], v[16:19]\n"
   "buffer_load_dwordx4 v[224:227], v9, s[44:47], s17 offen offset:1024 sc1\n"
   "v_mfma_f32_16x16x32_bf16 v[20:23], a[212:215], v[136:139], v[20:23]\n"
   "s_waitcnt vmcnt(13)\n"
   "v_xor_b32 v140, v14, v140\n"
   "v_xor_b32_e64 v141, v15, v141\n"
   "v_mfma_f32_16x16x32_bf16 v[24:27], a[216:219], v[136:139], v[24:27]\n"
   "v_xor_b32 v142, v114, v142\n"
   "v_xor_b32 v143, v115, v143\n"
   "v_mfma_f32_16x16x32_bf16 v[28:31], a[220:223], v[136:139], v[28:31]\n"
   "v_or3_b32 v1, v140, v141, v1\n"
   "v_or3_b32 v1, v142, v143, v1\n"
   "v_mfma_f32_16x16x32_bf16 v[16:19], a[224:227], v[140:143], v[16:19]\n"
   "buffer_load_dwordx4 v[228:231], v9, s[44:47], s17 offen offset:2048 sc1\n"
   "v_mfma_f32_16x16x32_bf16 v[20:23], a[228:231], v[140:143], v[20:23]\n"
   "s_waitcnt vmcnt(13)\n"
   "v_xor_b32 v144, v14, v144\n"
   "v_xor_b32_e64 v145, v15, v145\n"
   "v_mfma_f32_16x16x32_bf16 v[24:27], a[232:235], v[140:143], v[24:27]\n"
   "v_xor_b32 v146, v114, v146\n"
   "v_xor_b32 v147, v115, v147\n"
   "v_mfma_f32_16x16x32_bf16 v[28:31], a[236:239], v[140:143], v[28:31]\n"
   "v_or3_b32 v1, v144, v145, v1\n"
   "v_or3_b32 v1, v146, v147, v1\n"
   "v_mfma_f32_16x16x32_bf16 v[16:19], a[240:243], v[144:147], v[16:19]\n"
   "buffer_load_dwordx4 v[244:247], v9, s[44:47], s17 offen offset:3072 sc1\n"
   "v_mfma_f32_16x16x32_bf16 v[20:23], a[244:247], v[144:147], v[20:23]\n"
   "s_waitcnt vmcnt(13)\n"
   "v_xor_b32 v148, v14, v148\n"
   "v_xor_b32_e64 v149, v15, v149\n"
   "v_mfma_f32_16x16x32_bf16 v[24:27], a[248:251], v[144:147], v[24:27]\n"
   "v_xor_b32 v150, v114, v150\n"
   "v_xor_b32 v151, v115, v151\n"
   "v_mfma_f32_16x16x32_bf16 v[28:31], a[252:255], v[144:147], v[28:31]\n"
   "v_or3_b32 v1, v148, v149, v1\n"
   "v_or3_b32 v1, v150, v151, v1\n"
   "v_mfma_f32_16x16x32_bf16 v[16:19], a[0:3], v[148:151], v[16:19]\n"
   "buffer_load_dwordx4 v[248:251], v6, s[44:47], s17 offen sc1\n"
   "v_mfma_f32_16x16x32_bf16 v[20:23], a[4:7], v[148:151], v[20:23]\n"
   "s_waitcnt vmcnt(13)\n"
   "v_xor_b32 v152, v14, v152\n"
   "v_xor_b32_e64 v153, v15, v153\n"
   "v_mfma_f32_16x16x32_bf16 v[24:27], a[8:11], v[148:151], v[24:27]\n"
   "v_xor_b32 v154, v114, v154\n"
   "v_xor_b32 v155, v115, v155\n"
   "v_mfma_f32_16x16x32_bf16 v[28:31], a[12:15], v[148:151], v[28:31]\n"
   "v_or3_b32 v1, v152, v153, v1\n"
   "v_or3_b32 v1, v154, v155, v1\n"
   "v_mfma_f32_16x16x32_bf16 v[16:19], a[16:19], v[152:155], v[16:19]\n"
   "buffer_load_dwordx4 v[92:95], v6, s[44:47], s17 offen offset:1024 sc1\n"
   "v_mfma_f32_16x16x32_bf16 v[20:23], a[20:23], v[152:155], v[20:23]\n"
   "s_waitcnt vmcnt(13)\n"
   "v_xor_b32 v156, v14, v156\n"
   "v_xor_b32_e64 v157, v15, v157\n"
   "v_mfma_f32_16x16x32_bf16 v[24:27], a[24:27], v[152:155], v[24:27]\n"
   "v_xor_b32 v158, v114, v158\n"
   "v_xor_b32 v159, v115, v159\n"
   "v_mfma_f32_16x16x32_bf16 v[28:31], a[28:31], v[152:155], v[28:31]\n"
   "v_or3_b32 v1, v156, v157, v1\n"
   "v_or3_b32 v1, v158, v159, v1\n"
   "v_mfma_f32_16x16x32_bf16 v[16:19], a[32:35], v[156:159], v[16:19]\n"
   "buffer_load_dwordx4 v[96:99], v6, s[44:47], s17 offen offset:2048 sc1\n"
   "v_mfma_f32_16x16x32_bf16 v[20:23], a[36:39], v[156:159], v[20:23]\n"
   "s_waitcnt vmcnt(13)\n"
   "v_xor_b32 v168, v14, v168\n"
   "v_xor_b32_e64 v169, v15, v169\n"
   "v_mfma_f32_16x16x32_bf16 v[24:27], a[40:43], v[156:159], v[24:27]\n"
   "v_xor_b32 v170, v114, v170\n"
   "v_xor_b32 v171, v115, v171\n"
   "v_mfma_f32_16x16x32_bf16 v[28:31], a[44:47], v[156:159], v[28:31]\n"
   "v_or3_b32 v1, v168, v169, v1\n"
   "v_or3_b32 v1, v170, v171, v1\n"
   "v_mfma_f32_16x16x32_bf16 v[16:19], a[48:51], v[168:171], v[16:19]\n"
   "buffer_load_dwordx4 v[100:103], v6, s[44:47], s17 offen offset:3072 sc1\n"
   "v_mfma_f32_16x16x32_bf16 v[20:23], a[52:55], v[168:171], v[20:23]\n"
   "s_waitcnt vmcnt(13)\n"
   "v_xor_b32 v184, v14, v184\n"
   "v_xor_b32_e64 v185, v15, v185\n"
   "v_mfma_f32_16x16x32_bf16 v[24:27], a[56:59], v[168:171], v[24:27]\n"
   "v_xor_b32 v186, v114, v186\n"
   "v_xor_b32 v187, v115, v187\n"
   "v_mfma_f32_16x16x32_bf16 v[28:31], a[60:63], v[168:171], v[28:31]\n"
   "v_or3_b32 v1, v184, v185, v1\n"
   "v_or3_b32 v1, v186, v187, v1\n"
   "v_mfma_f32_16x16x32_bf16 v[16:19], a[64:67], v[184:187], v[16:19]\n"
   "buffer_load_dwordx4 v[104:107], v162, s[44:47], s17 offen sc1\n"
   "v_mfma_f32_16x16x32_bf16 v[20:23], a[68:71], v[184:187], v[20:23]\n"
   "s_waitcnt vmcnt(13)\n"
   "v_xor_b32 v200, v14, v200\n"
   "v_xor_b32_e64 v201, v15, v201\n"
   "v_mfma_f32_16x16x32_bf16 v[24:27], a[72:75], v[184:187], v[24:27]\n"
   "v_xor_b32 v202, v114, v202\n"
   "v_xor_b32 v203, v115, v203\n"
   "v_mfma_f32_16x16x32_bf16 v[28:31], a[76:79], v[184:187], v[28:31]\n"
   "v_or3_b32 v1, v200, v201, v1\n"
   "v_or3_b32 v1, v202, v203, v1\n"
   "v_mfma_f32_16x16x32_bf16 v[16:19], a[80:83], v[200:203], v[16:19]\n"
   "buffer_load_dwordx4 v[108:111], v162, s[44:47], s17 offen offset:1024 sc1\n"
   "v_mfma_f32_16x16x32_bf16 v[20:23], a[84:87], v[200:203], v[20:23]\n"
   "s_waitcnt vmcnt(13)\n"
   "v_xor_b32 v204, v14, v204\n"
   "v_xor_b32_e64 v205, v15, v205\n"
   "v_mfma_f32_16x16x32_bf16 v[24:27], a[88:91], v[200:203], v[24:27]\n"
   "v_xor_b32 v206, v114, v206\n"
   "v_xor_b32 v207, v115, v207\n"
   "v_mfma_f32_16x16x32_bf16 v[28:31], a[92:95], v[200:203], v[28:31]\n"
   "s_waitcnt lgkmcnt(6)\n"
   "v_or3_b32 v1, v204, v205, v1\n"
   "v_or3_b32 v1, v206, v207, v1\n"
   "v_mfma_f32_16x16x32_bf16 v[16:19], a[96:99], v[204:207], v[16:19]\n"
   "s_waitcnt lgkmcnt(4)\n"
   "buffer_load_dwordx4 v[116:119], v163, s[44:47], s17 offen offset:2048 sc1\n"
   "v_mfma_f32_16x16x32_bf16 v[20:23], a[100:103], v[204:207], v[20:23]\n"
   "s_waitcnt vmcnt(13)\n"
   "v_xor_b32 v212, v14, v212\n"
   "v_xor_b32 v213, v15, v213\n"
   "s_waitcnt lgkmcnt(2)\n"
   "v_mfma_f32_16x16x32_bf16 v[24:27], a[104:107], v[204:207], v[24:27]\n"
   "s_waitcnt lgkmcnt(0)\n"
   "v_xor_b32 v214, v114, v214\n"
   "v_xor_b32_e64 v215, v115, v215\n"
   "v_mfma_f32_16x16x32_bf16 v[28:31], a[108:111], v[204:207], v[28:31]\n"
   "v_or3_b32 v2, v212, v213, v213\n"
   "v_or3_b32 v2, v214, v215, v2\n"
   "v_mfma_f32_16x16x32_bf16 v[32:35], a[192:195], v[212:215], 0\n"
   "v_mfma_f32_16x16x32_bf16 v[36:39], a[196:199], v[212:215], 0\n"
   "s_waitcnt vmcnt(9)\n"
   "v_xor_b32 v224, v14, v224\n"
   "v_xor_b32_e64 v225, v15, v225\n"
   "v_mfma_f32_16x16x32_bf16 v[40:43], a[200:203], v[212:215], 0\n"
   "v_xor_b32 v226, v114, v226\n"
   "v_xor_b32 v227, v115, v227\n"
   "v_mfma_f32_16x16x32_bf16 v[44:47], a[204:207], v[212:215], 0\n"
   "v_or3_b32 v2, v224, v225, v2\n"
   "v_or3_b32 v2, v226, v227, v2\n"
   "v_mfma_f32_16x16x32_bf16 v[32:35], a[208:211], v[224:227], v[32:35]\n"
   "v_and_b32 v1, 0x80008000, v1\n"
   "v_mfma_f32_16x16x32_bf16 v[36:39], a[212:215], v[224:227], v[36:39]\n"
   "s_waitcnt vmcnt(8)\n"
   "v_xor_b32 v228, v14, v228\n"
   "v_cmp_ne_u32 vcc, 0, v1\n"
   "v_xor_b32 v229, v15, v229\n"
   "v_mfma_f32_16x16x32_bf16 v[40:43], a[216:219], v[224:227], v[40:43]\n"
   "s_cbranch_vccnz .Lfb_stub34\n"
   ".Lfb_back35:\n"
   "v_xor_b32 v230, v114, v230\n"
   "v_xor_b32 v231, v115, v231\n"
   "v_mfma_f32_16x16x32_bf16 v[44:47], a[220:223], v[224:227], v[44:47]\n"
   "v_or3_b32 v2, v228, v229, v2\n"
   "v_or3_b32 v2, v230, v231, v2\n"
   "v_mfma_f32_16x16x32_bf16 v[32:35], a[224:227], v[228:231], v[32:35]\n"
   "buffer_load_dwordx4 v[132:135], v9, s[44:47], s18 offen sc1\n"
   "v_mfma_f32_16x16x32_bf16 v[36:39], a[228:231], v[228:231], v[36:39]\n"
   "s_waitcnt vmcnt(8)\n"
   "v_xor_b32 v244, v14, v244\n"
   "v_xor_b32_e64 v245, v15, v245\n"
   "v_mfma_f32_16x16x32_bf16 v[40:43], a[232:235], v[228:231], v[40:43]\n"
   "ds_write_b128 v164, v[16:19]\n"
   "v_xor_b32 v246, v114, v246\n"
   "v_xor_b32 v247, v115, v247\n"
   "v_mfma_f32_16x16x32_bf16 v[44:47], a[236:239], v[228:231], v[44:47]\n"
   "v_or3_b32 v2, v244, v245, v2\n"
   "v_or3_b32 v2, v246, v247, v2\n"
   "v_mfma_f32_16x16x32_bf16 v[32:35], a[240:243], v[244:247], v[32:35]\n"
   "buffer_load_dwordx4 v[136:139], v9, s[44:47], s18 offen offset:1024 sc1\n"
   "v_mfma_f32_16x16x32_bf16 v[36:39], a[244:247], v[244:247], v[36:39]\n"
   "s_waitcnt vmcnt(8)\n"
   "v_xor_b32 v248, v14, v248\n"
   "v_xor_b32_e64 v249, v15, v249\n"
   "v_mfma_f32_16x16x32_bf16 v[40:43], a[248:251], v[244:247], v[40:43]\n"
   "ds_write_b128 v164, v[20:23] offset:1024\n"
   "v_xor_b32 v250, v114, v250\n"
   "v_xor_b32 v251, v115, v251\n"
   "v_mfma_f32_16x16x32_bf16 v[44:47], a[252:255], v[244:247], v[44:47]\n"
   "v_or3_b32 v2, v248, v249, v2\n"
   "v_or3_b32 v2, v250, v251, v2\n"
   "v_mfma_f32_16x16x32_bf16 v[32:35], a[0:3], v[248:251], v[32:35]\n"
   "buffer_load_dwordx4 v[140:143], v9, s[44:47], s18 offen offset:2048 sc1\n"
   "buffer_load_dwordx4 v[144:147], v9, s[44:47], s18 offen offset:3072 sc1\n"
   "v_mfma_f32_16x16x32_bf16 v[36:39], a[4:7], v[248:251], v[36:39]\n"
   "s_waitcnt vmcnt(9)\n"
   "v_xor_b32 v92, v14, v92\n"
   "v_xor_b32_e64 v93, v15, v93\n"
   "v_mfma_f32_16x16x32_bf16 v[40:43], a[8:11], v[248:251], v[40:43]\n"
   "ds_write_b128 v165, v[24:27]\n"
   "v_xor_b32 v94, v114, v94\n"
   "v_xor_b32 v95, v115, v95\n"
   "v_mfma_f32_16x16x32_bf16 v[44:47], a[12:15], v[248:251], v[44:47]\n"
   "v_or3_b32 v2, v92, v93, v2\n"
   "v_or3_b32 v2, v94, v95, v2\n"
   "v_mfma_f32_16x16x32_bf16 v[32:35], a[16:19], v[92:95], v[32:35]\n"
   "buffer_load_dwordx4 v[148:151], v6, s[44:47], s18 offen sc1\n"
   "v_mfma_f32_16x16x32_bf16 v[36:39], a[20:23], v[92:95], v[36:39]\n"
   "s_waitcnt vmcnt(9)\n"
   "v_xor_b32 v96, v14, v96\n"
   "v_xor_b32_e64 v97, v15, v97\n"
   "v_mfma_f32_16x16x32_bf16 v[40:43], a[24:27], v[92:95], v[40:43]\n"
   "ds_write_b128 v165, v[28:31] offset:1024\n"
   "v_xor_b32 v98, v114, v98\n"
   "v_xor_b32 v99, v115, v99\n"
   "v_mfma_f32_16x16x32_bf16 v[44:47], a[28:31], v[92:95], v[44:47]\n"
   "v_or3_b32 v2, v96, v97, v2\n"
   "v_or3_b32 v2, v98, v99, v2\n"
   "v_mfma_f32_16x16x32_bf16 v[32:35], a[32:35], v[96:99], v[32:35]\n"
   "buffer_load_dwordx4 v[152:155], v6, s[44:47], s18 offen offset:1024 sc1\n"
   "v_mfma_f32_16x16x32_bf16 v[36:39], a[36:39], v[96:99], v[36:39]\n"
   "s_waitcnt vmcnt(9)\n"
   "v_xor_b32 v100, v14, v100\n"
   "v_xor_b32_e64 v101, v15, v101\n"
   "v_mfma_f32_16x16x32_bf16 v[40:43], a[40:43], v[96:99], v[40:43]\n"
   "v_xor_b32 v102, v114, v102\n"
   "v_xor_b32 v103, v115, v103\n"
   "v_mfma_f32_16x16x32_bf16 v[44:47], a[44:47], v[96:99], v[44:47]\n"
   "v_or3_b32 v2, v100, v101, v2\n"
   "v_or3_b32 v2, v102, v103, v2\n"
   "v_mfma_f32_16x16x32_bf16 v[32:35], a[48:51], v[100:103], v[32:35]\n"
   "buffer_load_dwordx4 v[156:159], v6, s[44:47], s18 offen offset:2048 sc1\n"
   "buffer_load_dwordx4 v[168:171], v6, s[44:47], s18 offen offset:3072 sc1\n"
   "v_mfma_f32_16x16x32_bf16 v[36:39], a[52:55], v[100:103], v[36:39]\n"
   "s_waitcnt vmcnt(10)\n"
   "v_xor_b32 v104, v14, v104\n"
   "v_xor_b32_e64 v105, v15, v105\n"
   "v_mfma_f32_16x16x32_bf16 v[40:43], a[56:59], v[100:103], v[40:43]\n"
   "v_xor_b32 v106, v114, v106\n"
   "v_xor_b32 v107, v115, v107\n"
   "v_mfma_f32_16x16x32_bf16 v[44:47], a[60:63], v[100:103], v[44:47]\n"
   "v_or3_b32 v2, v104, v105, v2\n"
   "v_or3_b32 v2, v106, v107, v2\n"
   "v_mfma_f32_16x16x32_bf16 v[32:35], a[64:67], v[104:107], v[32:35]\n"
   "buffer_load_dwordx4 v[184:187], v162, s[44:47], s18 offen sc1\n"
   "v_mfma_f32_16x16x32_bf16 v[36:39], a[68:71], v[104:107], v[36:39]\n"
   "s_waitcnt vmcnt(10)\n"
   "v_xor_b32 v108, v14, v108\n"
   "v_xor_b32_e64 v109, v15, v109\n"
   "v_mfma_f32_16x16x32_bf16 v[40:43], a[72:75], v[104:107], v[40:43]\n"
   "v_xor_b32 v110, v114, v110\n"
   "v_xor_b32 v111, v115, v111\n"
   "v_mfma_f32_16x16x32_bf16 v[44:47], a[76:79], v[104:107], v[44:47]\n"
   "v_or3_b32 v2, v108, v109, v2\n"
   "v_or3_b32 v2, v110, v111, v2\n"
   "v_mfma_f32_16x16x32_bf16 v[32:35], a[80:83], v[108:111], v[32:35]\n"
   "buffer_load_dwordx4 v[200:203], v162, s[44:47], s18 offen offset:1024 sc1\n"
   "v_mfma_f32_16x16x32_bf16 v[36:39], a[84:87], v[108:111], v[36:39]\n"
   "s_waitcnt vmcnt(10)\n"
   "v_xor_b32 v116, v14, v116\n"
   "v_xor_b32_e64 v117, v15, v117\n"
   "v_mfma_f32_16x16x32_bf16 v[40:43], a[88:91], v[108:111], v[40:43]\n"
   "v_xor_b32 v118, v114, v118\n"
   "v_xor_b32 v119, v115, v119\n"
   "v_mfma_f32_16x16x32_bf16 v[44:47], a[92:95], v[108:111], v[44:47]\n"
   "v_or3_b32 v2, v116, v117, v2\n"
   "v_or3_b32 v2, v118, v119, v2\n"
   "v_mfma_f32_16x16x32_bf16 v[32:35], a[96:99], v[116:119], v[32:35]\n"
   "buffer_load_dwordx4 v[204:207], v163, s[44:47], s18 offen offset:2048 sc1\n"
   "v_mfma_f32_16x16x32_bf16 v[36:39], a[100:103], v[116:119], v[36:39]\n"
   "s_waitcnt vmcnt(10)\n"
   "v_xor_b32 v132, v14, v132\n"
   "v_xor_b32_e64 v133, v15, v133\n"
   "v_mfma_f32_16x16x32_bf16 v[40:43], a[104:107], v[116:119], v[40:43]\n"
   "v_xor_b32 v134, v114, v134\n"
   "v_xor_b32 v135, v115, v135\n"
   "v_mfma_f32_16x16x32_bf16 v[44:47], a[108:111], v[116:119], v[44:47]\n"
   "v_or3_b32 v1, v132, v133, v133\n"
   "v_or3_b32 v1, v134, v135, v1\n"
   "v_mfma_f32_16x16x32_bf16 v[16:19], a[192:195], v[132:135], 0\n"
   "buffer_load_dwordx4 v[212:215], v9, s[44:47], s19 offen sc1\n"
   "v_mfma_f32_16x16x32_bf16 v[20:23], a[196:199], v[132:135], 0\n"
   "s_waitcnt vmcnt(10)\n"
   "v_xor_b32 v136, v14, v136\n"
   "v_xor_b32_e64 v137, v15, v137\n"
   "v_mfma_f32_16x16x32_bf16 v[24:27], a[200:203], v[132:135], 0\n"
   "v_xor_b32 v138, v114, v138\n"
   "v_xor_b32 v139, v115, v139\n"
   "v_mfma_f32_16x16x32_bf16 v[28:31], a[204:207], v[132:135], 0\n"
   "v_or3_b32 v1, v136, v137, v1\n"
   "v_or3_b32 v1, v138, v139, v1\n"
   "v_mfma_f32_16x16x32_bf16 v[16:19], a[208:211], v[136:139], v[16:19]\n"
   "buffer_load_dwordx4 v[224:227], v9, s[44:47], s19 offen offset:1024 sc1\n"
   "v_mfma_f32_16x16x32_bf16 v[20:23], a[212:215], v[136:139], v[20:23]\n"
   "v_and_b32 v2, 0x80008000, v2\n"
   "s_waitcnt vmcnt(10)\n"
   "v_xor_b32 v140, v14, v140\n"
   "v_xor_b32_e64 v141, v15, v141\n"
   "v_mfma_f32_16x16x32_bf16 v[24:27], a[216:219], v[136:139], v[24:27]\n"
   "v_cmp_ne_u32 vcc, 0, v2\n"
   "v_xor_b32 v142, v114, v142\n"
   "v_xor_b32_e64 v143, v115, v143\n"
   "v_mfma_f32_16x16x32_bf16 v[28:31], a[220:223], v[136:139], v[28:31]\n"
   "s_cbranch_vccnz .Lfb_stub37\n"
   ".Lfb_back38:\n"
   "v_or3_b32 v1, v140, v141, v1\n"
   "v_or3_b32 v1, v142, v143, v1\n"
   "v_mfma_f32_16x16x32_bf16 v[16:19], a[224:227], v[140:143], v[16:19]\n"
   "buffer_load_dwordx4 v[228:231], v9, s[44:47], s19 offen offset:2048 sc1\n"
   "v_mfma_f32_16x16x32_bf16 v[20:23], a[228:231], v[140:143], v[20:23]\n"
   "s_waitcnt vmcnt(10)\n"
   "v_xor_b32 v144, v14, v144\n"
   "v_xor_b32_e64 v145, v15, v145\n"
   "v_mfma_f32_16x16x32_bf16 v[24:27], a[232:235], v[140:143], v[24:27]\n"
   "ds_write_b128 v180, v[40:43]\n"
   "v_xor_b32 v146, v114, v146\n"
   "v_xor_b32 v147, v115, v147\n"
   "v_mfma_f32_16x16x32_bf16 v[28:31], a[236:239], v[140:143], v[28:31]\n"
   "v_or3_b32 v1, v144, v145, v1\n"
   "v_or3_b32 v1, v146, v147, v1\n"
   "v_mfma_f32_16x16x32_bf16 v[16:19], a[240:243], v[144:147], v[16:19]\n"
   "buffer_load_dwordx4 v[244:247], v9, s[44:47], s19 offen offset:3072 sc1\n"
   "v_mfma_f32_16x16x32_bf16 v[20:23], a[244:247], v[144:147], v[20:23]\n"
   "s_waitcnt vmcnt(10)\n"
   "v_xor_b32 v148, v14, v148\n"
   "v_xor_b32_e64 v149, v15, v149\n"
   "v_mfma_f32_16x16x32_bf16 v[24:27], a[248:251], v[144:147], v[24:27]\n"
   "ds_write_b128 v180, v[44:47] offset:1024\n"
   "v_xor_b32 v150, v114, v150\n"
   "v_xor_b32 v151, v115, v151\n"
   "v_mfma_f32_16x16x32_bf16 v[28:31], a[252:255], v[144:147], v[28:31]\n"
   "v_or3_b32 v1, v148, v149, v1\n"
   "v_or3_b32 v1, v150, v151, v1\n"
   "v_mfma_f32_16x16x32_bf16 v[16:19], a[0:3], v[148:151], v[16:19]\n"
   "buffer_load_dwordx4 v[248:251], v6, s[44:47], s19 offen sc1\n"
   "v_mfma_f32_16x16x32_bf16 v[20:23], a[4:7], v[148:151], v[20:23]\n"
   "s_waitcnt vmcnt(10)\n"
   "v_xor_b32 v152, v14, v152\n"
   "v_xor_b32_e64 v153, v15, v153\n"
   "v_mfma_f32_16x16x32_bf16 v[24:27], a[8:11], v[148:151], v[24:27]\n"
   "v_xor_b32 v154, v114, v154\n"
   "v_xor_b32 v155, v115, v155\n"
   "v_mfma_f32_16x16x32_bf16 v[28:31], a[12:15], v[148:151], v[28:31]\n"
   "v_or3_b32 v1, v152, v153, v1\n"
   "v_or3_b32 v1, v154, v155, v1\n"
   "s_waitcnt lgkmcnt(0)\n"
   "s_barrier\n"
   "v_mfma_f32_16x16x32_bf16 v[16:19], a[16:19], v[152:155], v[16:19]\n"
   "ds_read_b128 v[64:67], v181\n"
   "ds_read_b128 v[68:71], v181 offset:1024\n"
   "buffer_load_dwordx4 v[92:95], v6, s[44:47], s19 offen offset:1024 sc1\n"
   "v_mfma_f32_16x16x32_bf16 v[20:23], a[20:23], v[152:155], v[20:23]\n"
   "ds_read_b128 v[72:75], v181 offset:8192\n"
   "s_waitcnt vmcnt(10)\n"
   "v_xor_b32 v156, v14, v156\n"
   "v_xor_b32_e64 v157, v15, v157\n"
   "v_mfma_f32_16x16x32_bf16 v[24:27], a[24:27], v[152:155], v[24:27]\n"
   "ds_read_b128 v[76:79], v181 offset:9216\n"
   "v_xor_b32 v158, v114, v158\n"
   "v_xor_b32 v159, v115, v159\n"
   "v_mfma_f32_16x16x32_bf16 v[28:31], a[28:31], v[152:155], v[28:31]\n"
   "ds_read_b128 v[80:83], v177\n"
   "v_or3_b32 v1, v156, v157, v1\n"
   "v_or3_b32 v1, v158, v159, v1\n"
   "v_mfma_f32_16x16x32_bf16 v[16:19], a[32:35], v[156:159], v[16:19]\n"
   "ds_read_b128 v[84:87], v177 offset:1024\n"
   "buffer_load_dwordx4 v[96:99], v6, s[44:47], s19 offen offset:2048 sc1\n"
   "v_mfma_f32_16x16x32_bf16 v[20:23], a[36:39], v[156:159], v[20:23]\n"
   "s_waitcnt vmcnt(10)\n"
   "v_xor_b32 v168, v14, v168\n"
   "s_waitcnt lgkmcnt(5)\n"
   "v_add_f32 v32, v32, v64\n"
   "v_add_f32_e64 v33, v33, v65\n"
   "v_mfma_f32_16x16x32_bf16 v[24:27], a[40:43], v[156:159], v[24:27]\n"
   "v_xor_b32 v169, v15, v169\n"
   "v_add_f32 v34, v34, v66\n"
   "v_xor_b32_e64 v170, v114, v170\n"
   "v_mfma_f32_16x16x32_bf16 v[28:31], a[44:47], v[156:159], v[28:31]\n"
   "v_add_f32 v35, v35, v67\n"
   "v_xor_b32 v171, v115, v171\n"
   "s_waitcnt lgkmcnt(4)\n"
   "v_add_f32 v36, v36, v68\n"
   "v_or3_b32 v1, v168, v169, v1\n"
   "v_or3_b32 v1, v170, v171, v1\n"
   "v_mfma_f32_16x16x32_bf16 v[16:19], a[48:51], v[168:171], v[16:19]\n"
   "buffer_load_dwordx4 v[100:103], v6, s[44:47], s19 offen offset:3072 sc1\n"
   "v_add_f32 v37, v37, v69\n"
   "v_add_f32 v38, v38, v70\n"
   "v_mfma_f32_16x16x32_bf16 v[20:23], a[52:55], v[168:171], v[20:23]\n"
   "v_add_f32 v39, v39, v71\n"
   "s_waitcnt lgkmcnt(3)\n"
   "v_add_f32 v32, v32, v72\n"
   "s_waitcnt vmcnt(10)\n"
   "v_xor_b32_e64 v184, v14, v184\n"
   "v_mfma_f32_16x16x32_bf16 v[24:27], a[56:59], v[168:171], v[24:27]\n"
   "v_add_f32 v33, v33, v73\n"
   "v_xor_b32 v185, v15, v185\n"
   "v_add_f32_e64 v34, v34, v74\n"
   "v_mfma_f32_16x16x32_bf16 v[28:31], a[60:63], v[168:171], v[28:31]\n"
   "v_xor_b32 v186, v114, v186\n"
   "v_add_f32 v35, v35, v75\n"
   "v_xor_b32_e64 v187, v115, v187\n"
   "v_or3_b32 v1, v184, v185, v1\n"
   "v_or3_b32 v1, v186, v187, v1\n"
   "v_mfma_f32_16x16x32_bf16 v[16:19], a[64:67], v[184:187], v[16:19]\n"
   "buffer_load_dwordx4 v[104:107], v162, s[44:47], s19 offen sc1\n"
   "s_waitcnt lgkmcnt(2)\n"
   "v_add_f32 v36, v36, v76\n"
   "v_add_f32 v37, v37, v77\n"
   "v_add_f32 v38, v38, v78\n"
   "v_mfma_f32_16x16x32_bf16 v[20:23], a[68:71], v[184:187], v[20:23]\n"
   "v_add_f32 v39, v39, v79\n"
   "s_waitcnt lgkmcnt(1)\n"
   "v_add_f32 v32, v32, v80\n"
   "s_waitcnt vmcnt(10)\n"
   "v_xor_b32_e64 v200, v14, v200\n"
   "v_mfma_f32_16x16x32_bf16 v[24:27], a[72:75], v[184:187], v[24:27]\n"
   "v_add_f32 v33, v33, v81\n"
   "v_xor_b32 v201, v15, v201\n"
   "v_add_f32_e64 v34, v34, v82\n"
   "v_mfma_f32_16x16x32_bf16 v[28:31], a[76:79], v[184:187], v[28:31]\n"
   "v_xor_b32 v202, v114, v202\n"
   "v_add_f32 v35, v35, v83\n"
   "v_xor_b32_e64 v203, v115, v203\n"
   "v_or3_b32 v1, v200, v201, v1\n"
   "v_or3_b32 v1, v202, v203, v1\n"
   "v_mfma_f32_16x16x32_bf16 v[16:19], a[80:83], v[200:203], v[16:19]\n"
   "buffer_load_dwordx4 v[108:111], v162, s[44:47], s19 offen offset:1024 sc1\n"
   "s_waitcnt lgkmcnt(0)\n"
   "v_add_f32 v36, v36, v84\n"
   "v_add_f32 v37, v37, v85\n"
   "v_add_f32 v38, v38, v86\n"
   "v_mfma_f32_16x16x32_bf16 v[20:23], a[84:87], v[200:203], v[20:23]\n"
   "v_add_f32_e64 v39, v39, v87\n"
   "v_fma_f32 v32, v32, v124, v48\n"
   "v_fma_f32 v33, v33, v125, v49\n"
   "v_mfma_f32_16x16x32_bf16 v[24:27], a[88:91], v[200:203], v[24:27]\n"
   "s_waitcnt vmcnt(10)\n"
   "v_xor_b32 v204, v14, v204\n"
   "v_fma_f32 v34, v34, v126, v50\n"
   "v_xor_b32_e64 v205, v15, v205\n"
   "v_mfma_f32_16x16x32_bf16 v[28:31], a[92:95], v[200:203], v[28:31]\n"
   "v_fma_f32 v35, v35, v127, v51\n"
   "v_xor_b32_e64 v206, v114, v206\n"
   "v_fma_f32 v36, v36, v128, v52\n"
   "v_xor_b32_e64 v207, v115, v207\n"
   "v_or3_b32 v1, v204, v205, v1\n"
   "v_or3_b32 v1, v206, v207, v1\n"
   "v_mfma_f32_16x16x32_bf16 v[16:19], a[96:99], v[204:207], v[16:19]\n"
   "buffer_load_dwordx4 v[116:119], v163, s[44:47], s19 offen offset:2048 sc1\n"
   "v_fma_f32 v37, v37, v129, v53\n"
   "v_fma_f32 v38, v38, v130, v54\n"
   "v_fma_f32 v39, v39, v131, v55\n"
   "v_mfma_f32_16x16x32_bf16 v[20:23], a[100:103], v[204:207], v[20:23]\n"
   "v_cvt_pk_bf16_f32 v88, v32, v33\n"
   "v_cvt_pk_bf16_f32 v90, v36, v37\n"
   "s_waitcnt vmcnt(10)\n"
   "v_xor_b32 v212, v14, v212\n"
   "v_mfma_f32_16x16x32_bf16 v[24:27], a[104:107], v[204:207], v[24:27]\n"
   "v_cvt_pk_bf16_f32 v89, v34, v35\n"
   "v_xor_b32_e64 v213, v15, v213\n"
   "v_cvt_pk_bf16_f32 v91, v38, v39\n"
   "v_mfma_f32_16x16x32_bf16 v[28:31], a[108:111], v[204:207], v[28:31]\n"
   "v_xor_b32 v214, v114, v214\n"
   "v_permlane16_swap_b32 v88, v90\n"
   "v_xor_b32 v215, v115, v215\n"
   "v_permlane16_swap_b32 v89, v91\n"
   "buffer_store_dwordx4 v[88:91], v113, s[40:43], s32 offen sc1\n"
   "s_add_u32 s32, s32, 0x8000\n"
   "v_or3_b32 v2, v212, v213, v213\n"
   "v_or3_b32 v2, v214, v215, v2\n"
   "v_mfma_f32_16x16x32_bf16 v[32:35], a[192:195], v[212:215], 0\n"
   "v_mfma_f32_16x16x32_bf16 v[36:39], a[196:199], v[212:215], 0\n"
   "s_waitcnt vmcnt(10)\n"
   "v_xor_b32 v224, v14, v224\n"
   "v_xor_b32_e64 v225, v15, v225\n"
   "v_mfma_f32_16x16x32_bf16 v[40:43], a[200:203], v[212:215], 0\n"
   "v_xor_b32 v226, v114, v226\n"
   "v_xor_b32 v227, v115, v227\n"
   "v_mfma_f32_16x16x32_bf16 v[44:47], a[204:207], v[212:215], 0\n"
   "v_or3_b32 v2, v224, v225, v2\n"
   "v_or3_b32 v2, v226, v227, v2\n"
   "v_mfma_f32_16x16x32_bf16 v[32:35], a[208:211], v[224:227], v[32:35]\n"
   "v_and_b32 v1, 0x80008000, v1\n"
   "v_mfma_f32_16x16x32_bf16 v[36:39], a[212:215], v[224:227], v[36:39]\n"
   "s_waitcnt vmcnt(9)\n"
   "v_xor_b32 v228, v14, v228\n"
   "v_cmp_ne_u32 vcc, 0, v1\n"
   "v_xor_b32 v229, v15, v229\n"
   "v_mfma_f32_16x16x32_bf16 v[40:43], a[216:219], v[224:227], v[40:43]\n"
   "s_cbranch_vccnz .Lfb_stub40\n"
   ".Lfb_back41:\n"
   "v_xor_b32 v230, v114, v230\n"
   "v_xor_b32 v231, v115, v231\n"
   "v_mfma_f32_16x16x32_bf16 v[44:47], a[220:223], v[224:227], v[44:47]\n"
   "v_or3_b32 v2, v228, v229, v2\n"
   "v_or3_b32 v2, v230, v231, v2\n"
   "v_mfma_f32_16x16x32_bf16 v[32:35], a[224:227], v[228:231], v[32:35]\n"
   "v_mfma_f32_16x16x32_bf16 v[36:39], a[228:231], v[228:231], v[36:39]\n"
   "s_waitcnt vmcnt(8)\n"
   "v_xor_b32 v244, v14, v244\n"
   "v_xor_b32_e64 v245, v15, v245\n"
   "v_mfma_f32_16x16x32_bf16 v[40:43], a[232:235], v[228:231], v[40:43]\n"
   "ds_write_b128 v164, v[16:19] offset:32768\n"
   "v_xor_b32 v246, v114, v246\n"
   "v_xor_b32 v247, v115, v247\n"
   "v_mfma_f32_16x16x32_bf16 v[44:47], a[236:239], v[228:231], v[44:47]\n"
   "v_or3_b32 v2, v244, v245, v2\n"
   "v_or3_b32 v2, v246, v247, v2\n"
   "v_mfma_f32_16x16x32_bf16 v[32:35], a[240:243], v[244:247], v[32:35]\n"
   "v_mfma_f32_16x16x32_bf16 v[36:39], a[244:247], v[244:247], v[36:39]\n"
   "s_waitcnt vmcnt(7)\n"
   "v_xor_b32 v248, v14, v248\n"
   "v_xor_b32_e64 v249, v15, v249\n"
   "v_mfma_f32_16x16x32_bf16 v[40:43], a[248:251], v[244:247], v[40:43]\n"
   "ds_write_b128 v164, v[20:23] offset:33792\n"
   "v_xor_b32 v250, v114, v250\n"
   "v_xor_b32 v251, v115, v251\n"
   "v_mfma_f32_16x16x32_bf16 v[44:47], a[252:255], v[244:247], v[44:47]\n"
   "v_or3_b32 v2, v248, v249, v2\n"
   "v_or3_b32 v2, v250, v251, v2\n"
   "v_mfma_f32_16x16x32_bf16 v[32:35], a[0:3], v[248:251], v[32:35]\n"
   "v_mfma_f32_16x16x32_bf16 v[36:39], a[4:7], v[248:251], v[36:39]\n"
   "s_waitcnt vmcnt(6)\n"
   "v_xor_b32 v92, v14, v92\n"
   "v_xor_b32_e64 v93, v15, v93\n"
   "v_mfma_f32_16x16x32_bf16 v[40:43], a[8:11], v[248:251], v[40:43]\n"
   "ds_write_b128 v165, v[24:27] offset:32768\n"
   "v_xor_b32 v94, v114, v94\n"
   "v_xor_b32 v95, v115, v95\n"
   "v_mfma_f32_16x16x32_bf16 v[44:47], a[12:15], v[248:251], v[44:47]\n"
   "v_or3_b32 v2, v92, v93, v2\n"
   "v_or3_b32 v2, v94, v95, v2\n"
   "v_mfma_f32_16x16x32_bf16 v[32:35], a[16:19], v[92:95], v[32:35]\n"
   "v_mfma_f32_16x16x32_bf16 v[36:39], a[20:23], v[92:95], v[36:39]\n"
   "s_waitcnt vmcnt(5)\n"
   "v_xor_b32 v96, v14, v96\n"
   "v_xor_b32_e64 v97, v15, v97\n"
   "v_mfma_f32_16x16x32_bf16 v[40:43], a[24:27], v[92:95], v[40:43]\n"
   "ds_write_b128 v165, v[28:31] offset:33792\n"
   "v_xor_b32 v98, v114, v98\n"
   "v_xor_b32 v99, v115, v99\n"
   "v_mfma_f32_16x16x32_bf16 v[44:47], a[28:31], v[92:95], v[44:47]\n"
   "v_or3_b32 v2, v96, v97, v2\n"
   "v_or3_b32 v2, v98, v99, v2\n"
   "v_mfma_f32_16x16x32_bf16 v[32:35], a[32:35], v[96:99], v[32:35]\n"
   "v_mfma_f32_16x16x32_bf16 v[36:39], a[36:39], v[96:99], v[36:39]\n"
   "s_waitcnt vmcnt(4)\n"
   "v_xor_b32 v100, v14, v100\n"
   "v_xor_b32_e64 v101, v15, v101\n"
   "v_mfma_f32_16x16x32_bf16 v[40:43], a[40:43], v[96:99], v[40:43]\n"
   "v_xor_b32 v102, v114, v102\n"
   "v_xor_b32 v103, v115, v103\n"
   "v_mfma_f32_16x16x32_bf16 v[44:47], a[44:47], v[96:99], v[44:47]\n"
   "v_or3_b32 v2, v100, v101, v2\n"
   "v_or3_b32 v2, v102, v103, v2\n"
   "s_waitcnt lgkmcnt(0)\n"
   "s_barrier\n"
   "v_mfma_f32_16x16x32_bf16 v[32:35], a[48:51], v[100:103], v[32:35]\n"
   "ds_read_b128 v[64:67], v181 offset:32768\n"
   "v_mfma_f32_16x16x32_bf16 v[36:39], a[52:55], v[100:103], v[36:39]\n"
   "ds_read_b128 v[68:71], v181 offset:33792\n"
   "s_waitcnt vmcnt(3)\n"
   "v_xor_b32 v104, v14, v104\n"
   "v_xor_b32_e64 v105, v15, v105\n"
   "v_mfma_f32_16x16x32_bf16 v[40:43], a[56:59], v[100:103], v[40:43]\n"
   "ds_read_b128 v[72:75], v181 offset:40960\n"
   "v_xor_b32 v106, v114, v106\n"
   "v_xor_b32 v107, v115, v107\n"
   "v_mfma_f32_16x16x32_bf16 v[44:47], a[60:63], v[100:103], v[44:47]\n"
   "ds_read_b128 v[76:79], v181 offset:41984\n"
   "v_or3_b32 v2, v104, v105, v2\n"
   "v_or3_b32 v2, v106, v107, v2\n"
   "v_mfma_f32_16x16x32_bf16 v[32:35], a[64:67], v[104:107], v[32:35]\n"
   "v_mfma_f32_16x16x32_bf16 v[36:39], a[68:71], v[104:107], v[36:39]\n"
   "s_waitcnt vmcnt(2)\n"
   "v_xor_b32 v108, v14, v108\n"
   "v_xor_b32_e64 v109, v15, v109\n"
   "v_mfma_f32_16x16x32_bf16 v[40:43], a[72:75], v[104:107], v[40:43]\n"
   "v_xor_b32 v110, v114, v110\n"
   "v_xor_b32 v111, v115, v111\n"
   "v_mfma_f32_16x16x32_bf16 v[44:47], a[76:79], v[104:107], v[44:47]\n"
   "v_or3_b32 v2, v108, v109, v2\n"
   "v_or3_b32 v2, v110, v111, v2\n"
   "v_mfma_f32_16x16x32_bf16 v[32:35], a[80:83], v[108:111], v[32:35]\n"
   "v_mfma_f32_16x16x32_bf16 v[36:39], a[84:87], v[108:111], v[36:39]\n"
   "s_waitcnt vmcnt(1)\n"
   "v_xor_b32 v116, v14, v116\n"
   "s_waitcnt lgkmcnt(1)\n"
   "v_add_f32 v64, v64, v72\n"
   "v_xor_b32_e64 v117, v15, v117\n"
   "v_mfma_f32_16x16x32_bf16 v[40:43], a[88:91], v[108:111], v[40:43]\n"
   "v_add_f32 v65, v65, v73\n"
   "v_xor_b32 v118, v114, v118\n"
   "v_xor_b32_e64 v119, v115, v119\n"
   "v_mfma_f32_16x16x32_bf16 v[44:47], a[92:95], v[108:111], v[44:47]\n"
   "v_add_f32_e64 v66, v66, v74\n"
   "v_or3_b32 v2, v116, v117, v2\n"
   "v_or3_b32 v2, v118, v119, v2\n"
   "v_mfma_f32_16x16x32_bf16 v[32:35], a[96:99], v[116:119], v[32:35]\n"
   "v_add_f32 v67, v67, v75\n"
   "s_waitcnt lgkmcnt(0)\n"
   "v_add_f32_e64 v68, v68, v76\n"
   "v_mfma_f32_16x16x32_bf16 v[36:39], a[100:103], v[116:119], v[36:39]\n"
   "v_add_f32_e64 v69, v69, v77\n"
   "v_mfma_f32_16x16x32_bf16 v[40:43], a[104:107], v[116:119], v[40:43]\n"
   "v_add_f32_e64 v70, v70, v78\n"
   "v_mfma_f32_16x16x32_bf16 v[44:47], a[108:111], v[116:119], v[44:47]\n"
   "v_add_f32 v71, v71, v79\n"
   "v_and_b32 v2, 0x80008000, v2\n"
   "v_cmp_ne_u32 vcc, 0, v2\n"
   "s_nop 0\n"
   "s_cbranch_vccnz .Lfb_stub43\n"
   ".Lfb_back44:\n"
   "s_nop 5\n"
   "ds_write_b128 v180, v[40:43] offset:32768\n"
   "ds_write_b128 v180, v[44:47] offset:33792\n"
   "v_pk_add_f32 v[32:33], v[32:33], v[64:65]\n"
   "v_pk_add_f32 v[34:35], v[34:35], v[66:67]\n"
   "v_pk_add_f32 v[36:37], v[36:37], v[68:69]\n"
   "v_pk_add_f32 v[38:39], v[38:39], v[70:71]\n"
   "s_waitcnt lgkmcnt(0)\n"
   "s_barrier\n"
   "ds_read_b128 v[80:83], v177 offset:32768\n"
   "ds_read_b128 v[84:87], v177 offset:33792\n"
   "s_waitcnt lgkmcnt(1)\n"
   "v_pk_add_f32 v[32:33], v[32:33], v[80:81]\n"
   "v_pk_add_f32 v[34:35], v[34:35], v[82:83]\n"
   "s_waitcnt lgkmcnt(0)\n"
   "v_pk_add_f32 v[36:37], v[36:37], v[84:85]\n"
   "v_pk_add_f32 v[38:39], v[38:39], v[86:87]\n"
   "v_pk_fma_f32 v[32:33], v[32:33], v[124:125], v[56:57]\n"
   "v_pk_fma_f32 v[34:35], v[34:35], v[126:127], v[58:59]\n"
   "v_pk_fma_f32 v[36:37], v[36:37], v[128:129], v[60:61]\n"
   "v_pk_fma_f32 v[38:39], v[38:39], v[130:131], v[62:63]\n"
   "v_cvt_pk_bf16_f32 v88, v32, v33\n"
   "v_cvt_pk_bf16_f32 v90, v36, v37\n"
   "v_cvt_pk_bf16_f32 v89, v34, v35\n"
   "v_cvt_pk_bf16_f32 v91, v38, v39\n"
   "v_permlane16_swap_b32 v88, v90\n"
   "s_nop 0\n"
   "v_permlane16_swap_b32 v89, v91\n"
   "buffer_store_dwordx4 v[88:91], v113, s[40:43], s32 offen sc1\n"
   "s_endpgm\n"
   ".Lfb_stub9:\n"
   "s_waitcnt lgkmcnt(0)\n"
   "s_lshr_b32 s60, s70, 5\n"
   "s_add_u32 s60, s60, 0xc0000\n"
   "buffer_load_dwordx4 v[224:227], v179, s[44:47], s60 offen sc1\n"
   "s_waitcnt vmcnt(0)\n"
   "v_xor_b32 v10, s48, v225\n"
   "v_bitop3_b32 v10, v227, s48, v10 bitop3:0xbe\n"
   "v_cmp_ne_u32 vcc, 0, v10\n"
   "s_waitcnt vmcnt(0) lgkmcnt(0)\n"
   "s_cbranch_vccz .Lfb_back10\n"
   "s_add_u32 s50, s50, 1\n"
   "s_cmp_lt_u32 s50, 0x30d40\n"
   "s_cbranch_scc1 .Lfb_stub9\n"
   "s_branch .Lfb_back10\n"
   ".Lfb_stub11:\n"
   "s_waitcnt lgkmcnt(0)\n"
   "s_add_u32 s60, s81, 0x20000\n"
   "buffer_load_dwordx4 v[16:19], v178, s[44:47], s60 offen sc1\n"
   "s_add_u32 s60, s81, 0x23000\n"
   "buffer_load_dwordx4 v[20:23], v178, s[44:47], s60 offen sc1\n"
   "s_add_u32 s60, s81, 0x26000\n"
   "buffer_load_dwordx4 v[24:27], v178, s[44:47], s60 offen sc1\n"
   "s_add_u32 s60, s81, 0x29000\n"
   "buffer_load_dwordx4 v[28:31], v178, s[44:47], s60 offen sc1\n"
   "s_add_u32 s60, s81, 0x2c000\n"
   "buffer_load_dwordx4 v[192:195], v178, s[44:47], s60 offen sc1\n"
   "s_add_u32 s60, s81, 0x2f000\n"
   "buffer_load_dwordx4 v[196:199], v178, s[44:47], s60 offen sc1\n"
   "s_add_u32 s60, s81, 0x32000\n"
   "buffer_load_dwordx4 v[216:219], v178, s[44:47], s60 offen sc1\n"
   "s_add_u32 s60, s81, 0x35000\n"
   "buffer_load_dwordx4 v[220:223], v178, s[44:47], s60 offen sc1\n"
   "s_waitcnt vmcnt(7)\n"
   "v_xor_b32 v16, s86, v16\n"
   "v_xor_b32 v18, s86, v18\n"
   "s_waitcnt vmcnt(6)\n"
   "v_xor_b32 v20, s86, v20\n"
   "v_xor_b32 v22, s86, v22\n"
   "v_pk_add_f32 v[200:201], v[16:17], v[20:21]\n"
   "v_pk_add_f32 v[202:203], v[18:19], v[22:23]\n"
   "s_waitcnt vmcnt(5)\n"
   "v_xor_b32 v24, s86, v24\n"
   "v_xor_b32_e64 v26, s86, v26\n"
   "v_pk_add_f32 v[200:201], v[200:201], v[24:25]\n"
   "v_pk_add_f32 v[202:203], v[202:203], v[26:27]\n"
   "s_waitcnt vmcnt(4)\n"
   "v_xor_b32 v28, s86, v28\n"
   "v_xor_b32_e64 v30, s86, v30\n"
   "v_pk_add_f32 v[200:201], v[200:201], v[28:29]\n"
   "v_pk_add_f32 v[202:203], v[202:203], v[30:31]\n"
   "s_waitcnt vmcnt(3)\n"
   "v_xor_b32 v192, s86, v192\n"
   "v_xor_b32_e64 v194, s86, v194\n"
   "v_pk_add_f32 v[200:201], v[200:201], v[192:193]\n"
   "v_pk_add_f32 v[202:203], v[202:203], v[194:195]\n"
   "s_waitcnt vmcnt(2)\n"
   "v_xor_b32 v196, s86, v196\n"
   "v_xor_b32_e64 v198, s86, v198\n"
   "v_pk_add_f32 v[200:201], v[200:201], v[196:197]\n"
   "v_pk_add_f32 v[202:203], v[202:203], v[198:199]\n"
   "s_waitcnt vmcnt(1)\n"
   "v_xor_b32 v216, s86, v216\n"
   "v_xor_b32_e64 v218, s86, v218\n"
   "v_pk_add_f32 v[200:201], v[200:201], v[216:217]\n"
   "v_pk_add_f32 v[202:203], v[202:203], v[218:219]\n"
   "s_waitcnt vmcnt(0)\n"
   "v_xor_b32 v220, s86, v220\n"
   "v_xor_b32_e64 v222, s86, v222\n"
   "v_pk_add_f32 v[200:201], v[200:201], v[220:221]\n"
   "v_pk_add_f32 v[202:203], v[202:203], v[222:223]\n"
   "v_cvt_pk_bf16_f32 v204, v200, v201\n"
   "v_cvt_pk_bf16_f32 v205, v202, v203\n"
   "v_lshlrev_b32 v200, 16, v204\n"
   "v_and_b32 v201, s65, v204\n"
   "v_lshlrev_b32 v202, 16, v205\n"
   "v_and_b32 v203, s65, v205\n"
   "v_pk_add_f32 v[200:201], v[200:201], v[180:181] op_sel_hi:[1,0]\n"
   "v_pk_add_f32 v[202:203], v[202:203], v[180:181] op_sel_hi:[1,0]\n"
   "v_cvt_pk_bf16_f32 v204, v200, v201\n"
   "v_cvt_pk_bf16_f32 v205, v202, v203\n"
   "v_lshlrev_b32 v200, 16, v204\n"
   "v_and_b32 v201, s65, v204\n"
   "v_lshlrev_b32 v202, 16, v205\n"
   "v_and_b32 v203, s65, v205\n"
   "ds_write_b128 v177, v[200:203] offset:2048\n"
   "ds_read_b128 v[228:231], v175 offset:2560\n"
   "ds_read_b128 v[232:235], v175 offset:2576\n"
   "ds_read_b128 v[236:239], v175 offset:2048\n"
   "ds_read_b128 v[240:243], v175 offset:2064\n"
   "v_or3_b32 v10, v16, v18, v20\n"
   "v_or3_b32 v10, v10, v22, v24\n"
   "v_or3_b32 v10, v10, v26, v28\n"
   "v_or3_b32 v10, v10, v30, v192\n"
   "v_or3_b32 v10, v10, v194, v196\n"
   "v_or3_b32 v10, v10, v198, v216\n"
   "v_or3_b32 v10, v10, v218, v220\n"
   "v_or3_b32 v10, v10, v222, v222\n"
   "v_and_b32_e64 v10, v15, v10\n"
   "v_cmp_ne_u32 vcc, 0, v10\n"
   "s_waitcnt vmcnt(0) lgkmcnt(0)\n"
   "s_cbranch_vccz .Lfb_back12\n"
   "s_add_u32 s50, s50, 1\n"
   "s_cmp_lt_u32 s50, 0x30d40\n"
   "s_cbranch_scc1 .Lfb_stub11\n"
   "s_branch .Lfb_back12\n"
   ".Lfb_stub14:\n"
   "s_waitcnt lgkmcnt(0)\n"
   "buffer_load_dwordx4 v[16:19], v9, s[44:47], s71 offen sc1\n"
   "buffer_load_dwordx4 v[20:23], v9, s[44:47], s71 offen offset:1024 sc1\n"
   "buffer_load_dwordx4 v[24:27], v9, s[44:47], s71 offen offset:2048 sc1\n"
   "buffer_load_dwordx4 v[28:31], v9, s[44:47], s71 offen offset:3072 sc1\n"
   "s_waitcnt vmcnt(3)\n"
   "v_xor_b32 v16, s86, v16\n"
   "v_xor_b32 v18, s86, v18\n"
   "s_waitcnt vmcnt(2)\n"
   "v_xor_b32 v20, s86, v20\n"
   "v_xor_b32 v22, s86, v22\n"
   "v_pk_add_f32 v[200:201], v[16:17], v[20:21]\n"
   "v_pk_add_f32 v[202:203], v[18:19], v[22:23]\n"
   "s_waitcnt vmcnt(1)\n"
   "v_xor_b32 v24, s86, v24\n"
   "v_xor_b32_e64 v26, s86, v26\n"
   "v_pk_add_f32 v[200:201], v[200:201], v[24:25]\n"
   "v_pk_add_f32 v[202:203], v[202:203], v[26:27]\n"
   "s_waitcnt vmcnt(0)\n"
   "v_xor_b32 v28, s86, v28\n"
   "v_xor_b32_e64 v30, s86, v30\n"
   "v_pk_add_f32 v[200:201], v[200:201], v[28:29]\n"
   "v_pk_add_f32 v[202:203], v[202:203], v[30:31]\n"
   "v_or3_b32 v10, v16, v18, v20\n"
   "v_or3_b32 v10, v10, v22, v24\n"
   "v_permlane32_swap_b32 v200, v202\n"
   "v_permlane32_swap_b32 v201, v203\n"
   "v_add_f32 v200, v200, v202\n"
   "v_add_f32 v201, v201, v203\n"
   "v_mov_b32 v204, v200\n"
   "v_mov_b32 v205, v201\n"
   "v_or3_b32 v10, v10, v26, v28\n"
   "v_or3_b32 v10, v10, v30, v30\n"
   "v_permlane16_swap_b32 v200, v204\n"
   "v_permlane16_swap_b32 v201, v205\n"
   "v_add_f32 v200, v200, v204\n"
   "v_add_f32 v201, v201, v205\n"
   "v_and_b32 v10, v15, v10\n"
   "v_cmp_ne_u32 vcc, 0, v10\n"
   "v_add_f32_dpp v200, v200, v200 row_ror:8 row_mask:0xf bank_mask:0xf\n"
   "v_add_f32_dpp v201, v201, v201 row_ror:8 row_mask:0xf bank_mask:0xf\n"
   "v_cvt_pk_bf16_f32 v224, v200, v201\n"
   "s_waitcnt vmcnt(0) lgkmcnt(0)\n"
   "s_cbranch_vccz .Lfb_back15\n"
   "s_add_u32 s50, s50, 1\n"
   "s_cmp_lt_u32 s50, 0x30d40\n"
   "s_cbranch_scc1 .Lfb_stub14\n"
   "s_branch .Lfb_back15\n"
   ".Lfb_stub17:\n"
   "s_waitcnt lgkmcnt(0)\n"
   "s_add_u32 s60, s91, 0x1f000\n"
   "buffer_load_dwordx4 v[80:83], v184, s[44:47], s60 offen sc1\n"
   "s_waitcnt vmcnt(0)\n"
   "v_xor_b32 v80, s86, v80\n"
   "v_xor_b32 v82, s86, v82\n"
   "v_or3_b32 v10, v80, v82, v82\n"
   "v_and_b32 v10, v15, v10\n"
   "v_cmp_ne_u32 vcc, 0, v10\n"
   "v_permlane32_swap_b32 v80, v82\n"
   "v_permlane32_swap_b32 v81, v83\n"
   "v_add_f32 v80, v80, v82\n"
   "v_add_f32 v81, v81, v83\n"
   "v_mov_b32 v204, v80\n"
   "v_mov_b32 v205, v81\n"
   "s_nop 0\n"
   "v_permlane16_swap_b32 v80, v204\n"
   "v_permlane16_swap_b32 v81, v205\n"
   "v_add_f32 v80, v80, v204\n"
   "v_add_f32 v81, v81, v205\n"
   "s_nop 0\n"
   "v_add_f32_dpp v80, v80, v80 row_ror:8 row_mask:0xf bank_mask:0xf\n"
   "v_add_f32_dpp v81, v81, v81 row_ror:8 row_mask:0xf bank_mask:0xf\n"
   "v_cvt_pk_bf16_f32 v224, v80, v81\n"
   "s_waitcnt vmcnt(0) lgkmcnt(0)\n"
   "s_cbranch_vccz .Lfb_back18\n"
   "s_add_u32 s50, s50, 1\n"
   "s_cmp_lt_u32 s50, 0x30d40\n"
   "s_cbranch_scc1 .Lfb_stub17\n"
   "s_branch .Lfb_back18\n"
   ".Lfb_stub20:\n"
   "s_waitcnt lgkmcnt(0)\n"
   "buffer_load_dwordx4 v[224:227], v188, s[44:47], s74 offen sc1\n"
   "buffer_load_dwordx4 v[16:19], v178, s[44:47], s81 offen sc1\n"
   "s_waitcnt vmcnt(1)\n"
   "v_cndmask_b32 v225, v208, v225, s[62:63]\n"
   "v_cndmask_b32 v227, v208, v227, s[62:63]\n"
   "v_xor_b32 v10, s48, v225\n"
   "v_bitop3_b32 v10, v227, s48, v10 bitop3:0xbe\n"
   "s_waitcnt vmcnt(0)\n"
   "v_bitop3_b32 v10, v17, s48, v10 bitop3:0xbe\n"
   "v_bitop3_b32 v10, v19, s48, v10 bitop3:0xbe\n"
   "v_cmp_ne_u32 vcc, 0, v10\n"
   "s_waitcnt vmcnt(0) lgkmcnt(0)\n"
   "s_cbranch_vccz .Lfb_back21\n"
   "s_add_u32 s50, s50, 1\n"
   "s_cmp_lt_u32 s50, 0x30d40\n"
   "s_cbranch_scc1 .Lfb_stub20\n"
   "s_branch .Lfb_back21\n"
   ".Lfb_stub23:\n"
   "s_waitcnt lgkmcnt(0)\n"
   "buffer_load_dwordx4 v[96:99], v177, s[44:47], s87 offen sc1\n"
   "buffer_load_dwordx4 v[100:103], v177, s[44:47], s87 offen offset:256 sc1\n"
   "buffer_load_dwordx4 v[104:107], v179, s[44:47], s87 offen offset:2048 sc1\n"
   "buffer_load_dwordx4 v[108:111], v179, s[44:47], s87 offen offset:2064 sc1\n"
   "s_waitcnt vmcnt(1)\n"
   "v_mov_b32 v92, v104\n"
   "v_mov_b32 v93, v106\n"
   "s_waitcnt vmcnt(0)\n"
   "v_mov_b32 v94, v108\n"
   "v_mov_b32 v95, v110\n"
   "v_mov_b32 v88, v96\n"
   "v_mov_b32 v89, v98\n"
   "v_mfma_f32_16x16x32_bf16 v[192:195], v[92:95], v[124:127], 0\n"
   "v_mov_b32 v90, v100\n"
   "v_mov_b32 v91, v102\n"
   "v_mfma_f32_16x16x32_bf16 v[196:199], v[92:95], v[128:131], 0\n"
   "v_xor_b32_e64 v10, s48, v97\n"
   "v_bitop3_b32 v10, v99, s48, v10 bitop3:0xbe\n"
   "v_mfma_f32_16x16x32_bf16 v[216:219], v[92:95], v[140:143], 0\n"
   "v_bitop3_b32 v10, v101, s48, v10 bitop3:0xbe\n"
   "v_bitop3_b32 v10, v103, s48, v10 bitop3:0xbe\n"
   "v_mfma_f32_16x16x32_bf16 v[220:223], v[92:95], v[144:147], 0\n"
   "v_bitop3_b32 v10, v105, s48, v10 bitop3:0xbe\n"
   "v_bitop3_b32 v10, v107, s48, v10 bitop3:0xbe\n"
   "v_mfma_f32_16x16x32_bf16 v[32:35], v[88:91], v[124:127], 0\n"
   "v_bitop3_b32 v10, v109, s48, v10 bitop3:0xbe\n"
   "v_bitop3_b32 v10, v111, s48, v10 bitop3:0xbe\n"
   "v_mfma_f32_16x16x32_bf16 v[36:39], v[88:91], v[128:131], 0\n"
   "v_cmp_ne_u32 vcc, 0, v10\n"
   "v_mfma_f32_16x16x32_bf16 v[40:43], v[88:91], v[140:143], 0\n"
   "v_mfma_f32_16x16x32_bf16 v[44:47], v[88:91], v[144:147], 0\n"
   "s_waitcnt vmcnt(0) lgkmcnt(0)\n"
   "s_cbranch_vccz .Lfb_back24\n"
   "s_add_u32 s50, s50, 1\n"
   "s_cmp_lt_u32 s50, 0x30d40\n"
   "s_cbranch_scc1 .Lfb_stub23\n"
   "s_branch .Lfb_back24\n"
   ".Lfb_stub27:\n"
   "s_waitcnt lgkmcnt(0)\n"
   "buffer_load_dwordx2 v[166:167], v112, s[44:47], s76 offen sc1\n"
   "s_waitcnt vmcnt(0)\n"
   "v_cmp_ne_u64 vcc, s[48:49], v[166:167]\n"
   "s_nop 0\n"
   "s_and_b64 vcc, vcc, s[88:89]\n"
   "s_waitcnt vmcnt(0) lgkmcnt(0)\n"
   "s_cbranch_vccz .Lfb_back28\n"
   "s_add_u32 s50, s50, 1\n"
   "s_cmp_lt_u32 s50, 0x30d40\n"
   "s_cbranch_scc1 .Lfb_stub27\n"
   "s_branch .Lfb_back28\n"
   ".Lfb_stub30:\n"
   "s_waitcnt lgkmcnt(0)\n"
   "buffer_load_dwordx2 v[166:167], v112, s[44:47], s76 offen sc1\n"
   "s_waitcnt vmcnt(0)\n"
   "v_cmp_ne_u64 vcc, s[48:49], v[166:167]\n"
   "s_nop 0\n"
   "s_and_b64 vcc, vcc, s[88:89]\n"
   "s_waitcnt vmcnt(0) lgkmcnt(0)\n"
   "s_cbranch_vccz .Lfb_back31\n"
   "s_add_u32 s50, s50, 1\n"
   "s_cmp_lt_u32 s50, 0x30d40\n"
   "s_cbranch_scc1 .Lfb_stub30\n"
   "s_branch .Lfb_back31\n"
   ".Lfb_stub32:\n"
   "s_waitcnt lgkmcnt(0)\n"
   "buffer_load_dwordx4 v[16:19], v6, s[44:47], s14 offen sc1\n"
   "buffer_load_dwordx4 v[20:23], v6, s[44:47], s15 offen sc1\n"
   "s_waitcnt vmcnt(1)\n"
   "v_xor_b32_e64 v10, s48, v17\n"
   "v_bitop3_b32 v10, v19, s48, v10 bitop3:0xbe\n"
   "s_waitcnt vmcnt(0)\n"
   "v_bitop3_b32 v10, v21, s48, v10 bitop3:0xbe\n"
   "v_bitop3_b32 v10, v23, s48, v10 bitop3:0xbe\n"
   "v_cmp_ne_u32 vcc, 0, v10\n"
   "s_waitcnt vmcnt(0) lgkmcnt(0)\n"
   "s_cbranch_vccz .Lfb_back33\n"
   "s_add_u32 s50, s50, 1\n"
   "s_cmp_lt_u32 s50, 0x30d40\n"
   "s_cbranch_scc1 .Lfb_stub32\n"
   "s_branch .Lfb_back33\n"
   ".Lfb_stub34:\n"
   "s_waitcnt lgkmcnt(0)\n"
   ".Lfb_end:\n"
   "buffer_load_dwordx4 v[120:123], v9, s[44:47], s16 offen sc1\n"
   "buffer_load_dwordx4 v[172:175], v9, s[44:47], s16 offen offset:1024 sc1\n"
   "buffer_load_dwordx4 v[188:191], v9, s[44:47], s16 offen offset:2048 sc1\n"
   "buffer_load_dwordx4 v[192:195], v9, s[44:47], s16 offen offset:3072 sc1\n"
   "buffer_load_dwordx4 v[196:199], v6, s[44:47], s16 offen sc1\n"
   "buffer_load_dwordx4 v[208:211], v6, s[44:47], s16 offen offset:1024 sc1\n"
   "buffer_load_dwordx4 v[216:219], v6, s[44:47], s16 offen offset:2048 sc1\n"
   "buffer_load_dwordx4 v[220:223], v6, s[44:47], s16 offen offset:3072 sc1\n"
   "buffer_load_dwordx4 v[232:235], v162, s[44:47], s16 offen sc1\n"
   "buffer_load_dwordx4 v[236:239], v162, s[44:47], s16 offen offset:1024 sc1\n"
   "buffer_load_dwordx4 v[240:243], v163, s[44:47], s16 offen offset:2048 sc1\n"
   "s_waitcnt vmcnt(10)\n"
   "v_xor_b32 v120, v14, v120\n"
   "v_xor_b32 v121, v15, v121\n"
   "v_xor_b32 v122, v114, v122\n"
   "v_xor_b32_e64 v123, v115, v123\n"
   "v_or3_b32 v1, v120, v121, v121\n"
   "v_or3_b32 v1, v122, v123, v1\n"
   "s_waitcnt vmcnt(9)\n"
   "v_xor_b32 v172, v14, v172\n"
   "v_xor_b32 v173, v15, v173\n"
   "v_xor_b32 v174, v114, v174\n"
   "v_xor_b32_e64 v175, v115, v175\n"
   "v_or3_b32 v1, v172, v173, v1\n"
   "v_or3_b32 v1, v174, v175, v1\n"
   "s_waitcnt vmcnt(8)\n"
   "v_xor_b32 v188, v14, v188\n"
   "v_xor_b32 v189, v15, v189\n"
   "v_xor_b32 v190, v114, v190\n"
   "v_xor_b32_e64 v191, v115, v191\n"
   "v_or3_b32 v1, v188, v189, v1\n"
   "v_or3_b32 v1, v190, v191, v1\n"
   "s_waitcnt vmcnt(7)\n"
   "v_xor_b32 v192, v14, v192\n"
   "v_xor_b32 v193, v15, v193\n"
   "v_xor_b32 v194, v114, v194\n"
   "v_xor_b32_e64 v195, v115, v195\n"
   "v_or3_b32 v1, v192, v193, v1\n"
   "v_or3_b32 v1, v194, v195, v1\n"
   "s_waitcnt vmcnt(6)\n"
   "v_xor_b32 v196, v14, v196\n"
   "v_xor_b32 v197, v15, v197\n"
   "v_xor_b32 v198, v114, v198\n"
   "v_xor_b32_e64 v199, v115, v199\n"
   "v_or3_b32 v1, v196, v197, v1\n"
   "v_or3_b32 v1, v198, v199, v1\n"
   "s_waitcnt vmcnt(5)\n"
   "v_xor_b32 v208, v14, v208\n"
   "v_xor_b32 v209, v15, v209\n"
   "v_xor_b32 v210, v114, v210\n"
   "v_xor_b32_e64 v211, v115, v211\n"
   "v_or3_b32 v1, v208, v209, v1\n"
   "v_or3_b32 v1, v210, v211, v1\n"
   "s_waitcnt vmcnt(4)\n"
   "v_xor_b32 v216, v14, v216\n"
   "v_xor_b32 v217, v15, v217\n"
   "v_xor_b32 v218, v114, v218\n"
   "v_xor_b32_e64 v219, v115, v219\n"
   "v_or3_b32 v1, v216, v217, v1\n"
   "v_or3_b32 v1, v218, v219, v1\n"
   "s_waitcnt vmcnt(3)\n"
   "v_xor_b32 v220, v14, v220\n"
   "v_xor_b32 v221, v15, v221\n"
   "v_xor_b32 v222, v114, v222\n"
   "v_xor_b32_e64 v223, v115, v223\n"
   "v_or3_b32 v1, v220, v221, v1\n"
   "v_or3_b32 v1, v222, v223, v1\n"
   "s_waitcnt vmcnt(2)\n"
   "v_xor_b32 v232, v14, v232\n"
   "v_xor_b32 v233, v15, v233\n"
   "v_xor_b32 v234, v114, v234\n"
   "v_xor_b32_e64 v235, v115, v235\n"
   "v_or3_b32 v1, v232, v233, v1\n"
   "v_or3_b32 v1, v234, v235, v1\n"
   "s_waitcnt vmcnt(1)\n"
   "v_xor_b32 v236, v14, v236\n"
   "v_xor_b32 v237, v15, v237\n"
   "v_xor_b32 v238, v114, v238\n"
   "v_xor_b32_e64 v239, v115, v239\n"
   "v_or3_b32 v1, v236, v237, v1\n"
   "v_or3_b32 v1, v238, v239, v1\n"
   "s_waitcnt vmcnt(0)\n"
   "v_xor_b32 v240, v14, v240\n"
   "v_xor_b32 v241, v15, v241\n"
   "v_xor_b32 v242, v114, v242\n"
   "v_xor_b32 v243, v115, v243\n"
   "v_or3_b32 v1, v240, v241, v1\n"
   "v_or3_b32 v1, v242, v243, v1\n"
   "v_and_b32 v1, 0x80008000, v1\n"
   "v_cmp_ne_u32 vcc, 0, v1\n"
   "s_waitcnt vmcnt(0) lgkmcnt(0)\n"
   "s_cbranch_vccz .Lfb_aft36\n"
   "s_add_u32 s50, s50, 1\n"
   "s_cmp_lt_u32 s50, 0x30d40\n"
   "s_cbranch_scc1 .Lfb_stub34\n"
   ".Lfb_aft36:\n"
   "v_mfma_f32_16x16x32_bf16 v[16:19], a[192:195], v[120:123], 0\n"
   "v_mfma_f32_16x16x32_bf16 v[20:23], a[196:199], v[120:123], 0\n"
   "v_mfma_f32_16x16x32_bf16 v[24:27], a[200:203], v[120:123], 0\n"
   "v_mfma_f32_16x16x32_bf16 v[28:31], a[204:207], v[120:123], 0\n"
   "v_mfma_f32_16x16x32_bf16 v[16:19], a[208:211], v[172:175], v[16:19]\n"
   "v_mfma_f32_16x16x32_bf16 v[20:23], a[212:215], v[172:175], v[20:23]\n"
   "v_mfma_f32_16x16x32_bf16 v[24:27], a[216:219], v[172:175], v[24:27]\n"
   "v_mfma_f32_16x16x32_bf16 v[28:31], a[220:223], v[172:175], v[28:31]\n"
   "v_mfma_f32_16x16x32_bf16 v[16:19], a[224:227], v[188:191], v[16:19]\n"
   "v_mfma_f32_16x16x32_bf16 v[20:23], a[228:231], v[188:191], v[20:23]\n"
   "v_mfma_f32_16x16x32_bf16 v[24:27], a[232:235], v[188:191], v[24:27]\n"
   "v_mfma_f32_16x16x32_bf16 v[28:31], a[236:239], v[188:191], v[28:31]\n"
   "v_mfma_f32_16x16x32_bf16 v[16:19], a[240:243], v[192:195], v[16:19]\n"
   "v_mfma_f32_16x16x32_bf16 v[20:23], a[244:247], v[192:195], v[20:23]\n"
   "v_mfma_f32_16x16x32_bf16 v[24:27], a[248:251], v[192:195], v[24:27]\n"
   "v_mfma_f32_16x16x32_bf16 v[28:31], a[252:255], v[192:195], v[28:31]\n"
   "v_mfma_f32_16x16x32_bf16 v[16:19], a[0:3], v[196:199], v[16:19]\n"
   "v_mfma_f32_16x16x32_bf16 v[20:23], a[4:7], v[196:199], v[20:23]\n"
   "v_mfma_f32_16x16x32_bf16 v[24:27], a[8:11], v[196:199], v[24:27]\n"
   "v_mfma_f32_16x16x32_bf16 v[28:31], a[12:15], v[196:199], v[28:31]\n"
   "v_mfma_f32_16x16x32_bf16 v[16:19], a[16:19], v[208:211], v[16:19]\n"
   "v_mfma_f32_16x16x32_bf16 v[20:23], a[20:23], v[208:211], v[20:23]\n"
   "v_mfma_f32_16x16x32_bf16 v[24:27], a[24:27], v[208:211], v[24:27]\n"
   "v_mfma_f32_16x16x32_bf16 v[28:31], a[28:31], v[208:211], v[28:31]\n"
   "v_mfma_f32_16x16x32_bf16 v[16:19], a[32:35], v[216:219], v[16:19]\n"
   "v_mfma_f32_16x16x32_bf16 v[20:23], a[36:39], v[216:219], v[20:23]\n"
   "v_mfma_f32_16x16x32_bf16 v[24:27], a[40:43], v[216:219], v[24:27]\n"
   "v_mfma_f32_16x16x32_bf16 v[28:31], a[44:47], v[216:219], v[28:31]\n"
   "v_mfma_f32_16x16x32_bf16 v[16:19], a[48:51], v[220:223], v[16:19]\n"
   "v_mfma_f32_16x16x32_bf16 v[20:23], a[52:55], v[220:223], v[20:23]\n"
   "v_mfma_f32_16x16x32_bf16 v[24:27], a[56:59], v[220:223], v[24:27]\n"
   "v_mfma_f32_16x16x32_bf16 v[28:31], a[60:63], v[220:223], v[28:31]\n"
   "v_mfma_f32_16x16x32_bf16 v[16:19], a[64:67], v[232:235], v[16:19]\n"
   "v_mfma_f32_16x16x32_bf16 v[20:23], a[68:71], v[232:235], v[20:23]\n"
   "v_mfma_f32_16x16x32_bf16 v[24:27], a[72:75], v[232:235], v[24:27]\n"
   "v_mfma_f32_16x16x32_bf16 v[28:31], a[76:79], v[232:235], v[28:31]\n"
   "v_mfma_f32_16x16x32_bf16 v[16:19], a[80:83], v[236:239], v[16:19]\n"
   "v_mfma_f32_16x16x32_bf16 v[20:23], a[84:87], v[236:239], v[20:23]\n"
   "v_mfma_f32_16x16x32_bf16 v[24:27], a[88:91], v[236:239], v[24:27]\n"
   "v_mfma_f32_16x16x32_bf16 v[28:31], a[92:95], v[236:239], v[28:31]\n"
   "v_mfma_f32_16x16x32_bf16 v[16:19], a[96:99], v[240:243], v[16:19]\n"
   "v_mfma_f32_16x16x32_bf16 v[20:23], a[100:103], v[240:243], v[20:23]\n"
   "v_mfma_f32_16x16x32_bf16 v[24:27], a[104:107], v[240:243], v[24:27]\n"
   "v_mfma_f32_16x16x32_bf16 v[28:31], a[108:111], v[240:243], v[28:31]\n"
   "s_branch .Lfb_back35\n"
   ".Lfb_stub37:\n"
   "s_waitcnt lgkmcnt(0)\n"
   "buffer_load_dwordx4 v[120:123], v9, s[44:47], s17 offen sc1\n"
   "s_nop 0\n"
   "buffer_load_dwordx4 v[172:175], v9, s[44:47], s17 offen offset:1024 sc1\n"
   "buffer_load_dwordx4 v[188:191], v9, s[44:47], s17 offen offset:2048 sc1\n"
   "buffer_load_dwordx4 v[192:195], v9, s[44:47], s17 offen offset:3072 sc1\n"
   "buffer_load_dwordx4 v[196:199], v6, s[44:47], s17 offen sc1\n"
   "buffer_load_dwordx4 v[208:211], v6, s[44:47], s17 offen offset:1024 sc1\n"
   "buffer_load_dwordx4 v[216:219], v6, s[44:47], s17 offen offset:2048 sc1\n"
   "buffer_load_dwordx4 v[220:223], v6, s[44:47], s17 offen offset:3072 sc1\n"
   "buffer_load_dwordx4 v[232:235], v162, s[44:47], s17 offen sc1\n"
   "buffer_load_dwordx4 v[236:239], v162, s[44:47], s17 offen offset:1024 sc1\n"
   "buffer_load_dwordx4 v[240:243], v163, s[44:47], s17 offen offset:2048 sc1\n"
   "s_waitcnt vmcnt(10)\n"
   "v_xor_b32 v120, v14, v120\n"
   "v_xor_b32 v121, v15, v121\n"
   "v_xor_b32 v122, v114, v122\n"
   "v_xor_b32_e64 v123, v115, v123\n"
   "v_or3_b32 v2, v120, v121, v121\n"
   "v_or3_b32 v2, v122, v123, v2\n"
   "s_waitcnt vmcnt(9)\n"
   "v_xor_b32 v172, v14, v172\n"
   "v_xor_b32 v173, v15, v173\n"
   "v_xor_b32 v174, v114, v174\n"
   "v_xor_b32_e64 v175, v115, v175\n"
   "v_or3_b32 v2, v172, v173, v2\n"
   "v_or3_b32 v2, v174, v175, v2\n"
   "s_waitcnt vmcnt(8)\n"
   "v_xor_b32 v188, v14, v188\n"
   "v_xor_b32 v189, v15, v189\n"
   "v_xor_b32 v190, v114, v190\n"
   "v_xor_b32_e64 v191, v115, v191\n"
   "v_or3_b32 v2, v188, v189, v2\n"
   "v_or3_b32 v2, v190, v191, v2\n"
   "s_waitcnt vmcnt(7)\n"
   "v_xor_b32 v192, v14, v192\n"
   "v_xor_b32 v193, v15, v193\n"
   "v_xor_b32 v194, v114, v194\n"
   "v_xor_b32_e64 v195, v115, v195\n"
   "v_or3_b32 v2, v192, v193, v2\n"
   "v_or3_b32 v2, v194, v195, v2\n"
   "s_waitcnt vmcnt(6)\n"
   "v_xor_b32 v196, v14, v196\n"
   "v_xor_b32 v197, v15, v197\n"
   "v_xor_b32 v198, v114, v198\n"
   "v_xor_b32_e64 v199, v115, v199\n"
   "v_or3_b32 v2, v196, v197, v2\n"
   "v_or3_b32 v2, v198, v199, v2\n"
   "s_waitcnt vmcnt(5)\n"
   "v_xor_b32 v208, v14, v208\n"
   "v_xor_b32 v209, v15, v209\n"
   "v_xor_b32 v210, v114, v210\n"
   "v_xor_b32_e64 v211, v115, v211\n"
   "v_or3_b32 v2, v208, v209, v2\n"
   "v_or3_b32 v2, v210, v211, v2\n"
   "s_waitcnt vmcnt(4)\n"
   "v_xor_b32 v216, v14, v216\n"
   "v_xor_b32 v217, v15, v217\n"
   "v_xor_b32 v218, v114, v218\n"
   "v_xor_b32_e64 v219, v115, v219\n"
   "v_or3_b32 v2, v216, v217, v2\n"
   "v_or3_b32 v2, v218, v219, v2\n"
   "s_waitcnt vmcnt(3)\n"
   "v_xor_b32 v220, v14, v220\n"
   "v_xor_b32 v221, v15, v221\n"
   "v_xor_b32 v222, v114, v222\n"
   "v_xor_b32_e64 v223, v115, v223\n"
   "v_or3_b32 v2, v220, v221, v2\n"
   "v_or3_b32 v2, v222, v223, v2\n"
   "s_waitcnt vmcnt(2)\n"
   "v_xor_b32 v232, v14, v232\n"
   "v_xor_b32 v233, v15, v233\n"
   "v_xor_b32 v234, v114, v234\n"
   "v_xor_b32_e64 v235, v115, v235\n"
   "v_or3_b32 v2, v232, v233, v2\n"
   "v_or3_b32 v2, v234, v235, v2\n"
   "s_waitcnt vmcnt(1)\n"
   "v_xor_b32 v236, v14, v236\n"
   "v_xor_b32 v237, v15, v237\n"
   "v_xor_b32 v238, v114, v238\n"
   "v_xor_b32_e64 v239, v115, v239\n"
   "v_or3_b32 v2, v236, v237, v2\n"
   "v_or3_b32 v2, v238, v239, v2\n"
   "s_waitcnt vmcnt(0)\n"
   "v_xor_b32 v240, v14, v240\n"
   "v_xor_b32 v241, v15, v241\n"
   "v_xor_b32 v242, v114, v242\n"
   "v_xor_b32 v243, v115, v243\n"
   "v_or3_b32 v2, v240, v241, v2\n"
   "v_or3_b32 v2, v242, v243, v2\n"
   "v_and_b32 v2, 0x80008000, v2\n"
   "v_cmp_ne_u32 vcc, 0, v2\n"
   "s_waitcnt vmcnt(0) lgkmcnt(0)\n"
   "s_cbranch_vccz .Lfb_aft39\n"
   "s_add_u32 s50, s50, 1\n"
   "s_cmp_lt_u32 s50, 0x30d40\n"
   "s_cbranch_scc1 .Lfb_stub37\n"
   ".Lfb_aft39:\n"
   "v_mfma_f32_16x16x32_bf16 v[32:35], a[192:195], v[120:123], 0\n"
   "v_mfma_f32_16x16x32_bf16 v[36:39], a[196:199], v[120:123], 0\n"
   "v_mfma_f32_16x16x32_bf16 v[40:43], a[200:203], v[120:123], 0\n"
   "v_mfma_f32_16x16x32_bf16 v[44:47], a[204:207], v[120:123], 0\n"
   "v_mfma_f32_16x16x32_bf16 v[32:35], a[208:211], v[172:175], v[32:35]\n"
   "v_mfma_f32_16x16x32_bf16 v[36:39], a[212:215], v[172:175], v[36:39]\n"
   "v_mfma_f32_16x16x32_bf16 v[40:43], a[216:219], v[172:175], v[40:43]\n"
   "v_mfma_f32_16x16x32_bf16 v[44:47], a[220:223], v[172:175], v[44:47]\n"
   "v_mfma_f32_16x16x32_bf16 v[32:35], a[224:227], v[188:191], v[32:35]\n"
   "v_mfma_f32_16x16x32_bf16 v[36:39], a[228:231], v[188:191], v[36:39]\n"
   "v_mfma_f32_16x16x32_bf16 v[40:43], a[232:235], v[188:191], v[40:43]\n"
   "v_mfma_f32_16x16x32_bf16 v[44:47], a[236:239], v[188:191], v[44:47]\n"
   "v_mfma_f32_16x16x32_bf16 v[32:35], a[240:243], v[192:195], v[32:35]\n"
   "v_mfma_f32_16x16x32_bf16 v[36:39], a[244:247], v[192:195], v[36:39]\n"
   "v_mfma_f32_16x16x32_bf16 v[40:43], a[248:251], v[192:195], v[40:43]\n"
   "v_mfma_f32_16x16x32_bf16 v[44:47], a[252:255], v[192:195], v[44:47]\n"
   "v_mfma_f32_16x16x32_bf16 v[32:35], a[0:3], v[196:199], v[32:35]\n"
   "v_mfma_f32_16x16x32_bf16 v[36:39], a[4:7], v[196:199], v[36:39]\n"
   "v_mfma_f32_16x16x32_bf16 v[40:43], a[8:11], v[196:199], v[40:43]\n"
   "v_mfma_f32_16x16x32_bf16 v[44:47], a[12:15], v[196:199], v[44:47]\n"
   "v_mfma_f32_16x16x32_bf16 v[32:35], a[16:19], v[208:211], v[32:35]\n"
   "v_mfma_f32_16x16x32_bf16 v[36:39], a[20:23], v[208:211], v[36:39]\n"
   "v_mfma_f32_16x16x32_bf16 v[40:43], a[24:27], v[208:211], v[40:43]\n"
   "v_mfma_f32_16x16x32_bf16 v[44:47], a[28:31], v[208:211], v[44:47]\n"
   "v_mfma_f32_16x16x32_bf16 v[32:35], a[32:35], v[216:219], v[32:35]\n"
   "v_mfma_f32_16x16x32_bf16 v[36:39], a[36:39], v[216:219], v[36:39]\n"
   "v_mfma_f32_16x16x32_bf16 v[40:43], a[40:43], v[216:219], v[40:43]\n"
   "v_mfma_f32_16x16x32_bf16 v[44:47], a[44:47], v[216:219], v[44:47]\n"
   "v_mfma_f32_16x16x32_bf16 v[32:35], a[48:51], v[220:223], v[32:35]\n"
   "v_mfma_f32_16x16x32_bf16 v[36:39], a[52:55], v[220:223], v[36:39]\n"
   "v_mfma_f32_16x16x32_bf16 v[40:43], a[56:59], v[220:223], v[40:43]\n"
   "v_mfma_f32_16x16x32_bf16 v[44:47], a[60:63], v[220:223], v[44:47]\n"
   "v_mfma_f32_16x16x32_bf16 v[32:35], a[64:67], v[232:235], v[32:35]\n"
   "v_mfma_f32_16x16x32_bf16 v[36:39], a[68:71], v[232:235], v[36:39]\n"
   "v_mfma_f32_16x16x32_bf16 v[40:43], a[72:75], v[232:235], v[40:43]\n"
   "v_mfma_f32_16x16x32_bf16 v[44:47], a[76:79], v[232:235], v[44:47]\n"
   "v_mfma_f32_16x16x32_bf16 v[32:35], a[80:83], v[236:239], v[32:35]\n"
   "v_mfma_f32_16x16x32_bf16 v[36:39], a[84:87], v[236:239], v[36:39]\n"
   "v_mfma_f32_16x16x32_bf16 v[40:43], a[88:91], v[236:239], v[40:43]\n"
   "v_mfma_f32_16x16x32_bf16 v[44:47], a[92:95], v[236:239], v[44:47]\n"
   "v_mfma_f32_16x16x32_bf16 v[32:35], a[96:99], v[240:243], v[32:35]\n"
   "v_mfma_f32_16x16x32_bf16 v[36:39], a[100:103], v[240:243], v[36:39]\n"
   "v_mfma_f32_16x16x32_bf16 v[40:43], a[104:107], v[240:243], v[40:43]\n"
   "v_mfma_f32_16x16x32_bf16 v[44:47], a[108:111], v[240:243], v[44:47]\n"
   "s_branch .Lfb_back38\n"
   ".Lfb_stub40:\n"
   "s_waitcnt lgkmcnt(0)\n"
   "buffer_load_dwordx4 v[120:123], v9, s[44:47], s18 offen sc1\n"
   "s_nop 0\n"
   "buffer_load_dwordx4 v[172:175], v9, s[44:47], s18 offen offset:1024 sc1\n"
   "buffer_load_dwordx4 v[188:191], v9, s[44:47], s18 offen offset:2048 sc1\n"
   "buffer_load_dwordx4 v[192:195], v9, s[44:47], s18 offen offset:3072 sc1\n"
   "buffer_load_dwordx4 v[196:199], v6, s[44:47], s18 offen sc1\n"
   "buffer_load_dwordx4 v[208:211], v6, s[44:47], s18 offen offset:1024 sc1\n"
   "buffer_load_dwordx4 v[216:219], v6, s[44:47], s18 offen offset:2048 sc1\n"
   "buffer_load_dwordx4 v[220:223], v6, s[44:47], s18 offen offset:3072 sc1\n"
   "buffer_load_dwordx4 v[232:235], v162, s[44:47], s18 offen sc1\n"
   "buffer_load_dwordx4 v[236:239], v162, s[44:47], s18 offen offset:1024 sc1\n"
   "buffer_load_dwordx4 v[240:243], v163, s[44:47], s18 offen offset:2048 sc1\n"
   "s_waitcnt vmcnt(10)\n"
   "v_xor_b32 v120, v14, v120\n"
   "v_xor_b32 v121, v15, v121\n"
   "v_xor_b32 v122, v114, v122\n"
   "v_xor_b32_e64 v123, v115, v123\n"
   "v_or3_b32 v1, v120, v121, v121\n"
   "v_or3_b32 v1, v122, v123, v1\n"
   "s_waitcnt vmcnt(9)\n"
   "v_xor_b32 v172, v14, v172\n"
   "v_xor_b32 v173, v15, v173\n"
   "v_xor_b32 v174, v114, v174\n"
   "v_xor_b32_e64 v175, v115, v175\n"
   "v_or3_b32 v1, v172, v173, v1\n"
   "v_or3_b32 v1, v174, v175, v1\n"
   "s_waitcnt vmcnt(8)\n"
   "v_xor_b32 v188, v14, v188\n"
   "v_xor_b32 v189, v15, v189\n"
   "v_xor_b32 v190, v114, v190\n"
   "v_xor_b32_e64 v191, v115, v191\n"
   "v_or3_b32 v1, v188, v189, v1\n"
   "v_or3_b32 v1, v190, v191, v1\n"
   "s_waitcnt vmcnt(7)\n"
   "v_xor_b32 v192, v14, v192\n"
   "v_xor_b32 v193, v15, v193\n"
   "v_xor_b32 v194, v114, v194\n"
   "v_xor_b32_e64 v195, v115, v195\n"
   "v_or3_b32 v1, v192, v193, v1\n"
   "v_or3_b32 v1, v194, v195, v1\n"
   "s_waitcnt vmcnt(6)\n"
   "v_xor_b32 v196, v14, v196\n"
   "v_xor_b32 v197, v15, v197\n"
   "v_xor_b32 v198, v114, v198\n"
   "v_xor_b32_e64 v199, v115, v199\n"
   "v_or3_b32 v1, v196, v197, v1\n"
   "v_or3_b32 v1, v198, v199, v1\n"
   "s_waitcnt vmcnt(5)\n"
   "v_xor_b32 v208, v14, v208\n"
   "v_xor_b32 v209, v15, v209\n"
   "v_xor_b32 v210, v114, v210\n"
   "v_xor_b32_e64 v211, v115, v211\n"
   "v_or3_b32 v1, v208, v209, v1\n"
   "v_or3_b32 v1, v210, v211, v1\n"
   "s_waitcnt vmcnt(4)\n"
   "v_xor_b32 v216, v14, v216\n"
   "v_xor_b32 v217, v15, v217\n"
   "v_xor_b32 v218, v114, v218\n"
   "v_xor_b32_e64 v219, v115, v219\n"
   "v_or3_b32 v1, v216, v217, v1\n"
   "v_or3_b32 v1, v218, v219, v1\n"
   "s_waitcnt vmcnt(3)\n"
   "v_xor_b32 v220, v14, v220\n"
   "v_xor_b32 v221, v15, v221\n"
   "v_xor_b32 v222, v114, v222\n"
   "v_xor_b32_e64 v223, v115, v223\n"
   "v_or3_b32 v1, v220, v221, v1\n"
   "v_or3_b32 v1, v222, v223, v1\n"
   "s_waitcnt vmcnt(2)\n"
   "v_xor_b32 v232, v14, v232\n"
   "v_xor_b32 v233, v15, v233\n"
   "v_xor_b32 v234, v114, v234\n"
   "v_xor_b32_e64 v235, v115, v235\n"
   "v_or3_b32 v1, v232, v233, v1\n"
   "v_or3_b32 v1, v234, v235, v1\n"
   "s_waitcnt vmcnt(1)\n"
   "v_xor_b32 v236, v14, v236\n"
   "v_xor_b32 v237, v15, v237\n"
   "v_xor_b32 v238, v114, v238\n"
   "v_xor_b32_e64 v239, v115, v239\n"
   "v_or3_b32 v1, v236, v237, v1\n"
   "v_or3_b32 v1, v238, v239, v1\n"
   "s_waitcnt vmcnt(0)\n"
   "v_xor_b32 v240, v14, v240\n"
   "v_xor_b32 v241, v15, v241\n"
   "v_xor_b32 v242, v114, v242\n"
   "v_xor_b32 v243, v115, v243\n"
   "v_or3_b32 v1, v240, v241, v1\n"
   "v_or3_b32 v1, v242, v243, v1\n"
   "v_and_b32 v1, 0x80008000, v1\n"
   "v_cmp_ne_u32 vcc, 0, v1\n"
   "s_waitcnt vmcnt(0) lgkmcnt(0)\n"
   "s_cbranch_vccz .Lfb_aft42\n"
   "s_add_u32 s50, s50, 1\n"
   "s_cmp_lt_u32 s50, 0x30d40\n"
   "s_cbranch_scc1 .Lfb_stub40\n"
   ".Lfb_aft42:\n"
   "v_mfma_f32_16x16x32_bf16 v[16:19], a[192:195], v[120:123], 0\n"
   "v_mfma_f32_16x16x32_bf16 v[20:23], a[196:199], v[120:123], 0\n"
   "v_mfma_f32_16x16x32_bf16 v[24:27], a[200:203], v[120:123], 0\n"
   "v_mfma_f32_16x16x32_bf16 v[28:31], a[204:207], v[120:123], 0\n"
   "v_mfma_f32_16x16x32_bf16 v[16:19], a[208:211], v[172:175], v[16:19]\n"
   "v_mfma_f32_16x16x32_bf16 v[20:23], a[212:215], v[172:175], v[20:23]\n"
   "v_mfma_f32_16x16x32_bf16 v[24:27], a[216:219], v[172:175], v[24:27]\n"
   "v_mfma_f32_16x16x32_bf16 v[28:31], a[220:223], v[172:175], v[28:31]\n"
   "v_mfma_f32_16x16x32_bf16 v[16:19], a[224:227], v[188:191], v[16:19]\n"
   "v_mfma_f32_16x16x32_bf16 v[20:23], a[228:231], v[188:191], v[20:23]\n"
   "v_mfma_f32_16x16x32_bf16 v[24:27], a[232:235], v[188:191], v[24:27]\n"
   "v_mfma_f32_16x16x32_bf16 v[28:31], a[236:239], v[188:191], v[28:31]\n"
   "v_mfma_f32_16x16x32_bf16 v[16:19], a[240:243], v[192:195], v[16:19]\n"
   "v_mfma_f32_16x16x32_bf16 v[20:23], a[244:247], v[192:195], v[20:23]\n"
   "v_mfma_f32_16x16x32_bf16 v[24:27], a[248:251], v[192:195], v[24:27]\n"
   "v_mfma_f32_16x16x32_bf16 v[28:31], a[252:255], v[192:195], v[28:31]\n"
   "v_mfma_f32_16x16x32_bf16 v[16:19], a[0:3], v[196:199], v[16:19]\n"
   "v_mfma_f32_16x16x32_bf16 v[20:23], a[4:7], v[196:199], v[20:23]\n"
   "v_mfma_f32_16x16x32_bf16 v[24:27], a[8:11], v[196:199], v[24:27]\n"
   "v_mfma_f32_16x16x32_bf16 v[28:31], a[12:15], v[196:199], v[28:31]\n"
   "v_mfma_f32_16x16x32_bf16 v[16:19], a[16:19], v[208:211], v[16:19]\n"
   "v_mfma_f32_16x16x32_bf16 v[20:23], a[20:23], v[208:211], v[20:23]\n"
   "v_mfma_f32_16x16x32_bf16 v[24:27], a[24:27], v[208:211], v[24:27]\n"
   "v_mfma_f32_16x16x32_bf16 v[28:31], a[28:31], v[208:211], v[28:31]\n"
   "v_mfma_f32_16x16x32_bf16 v[16:19], a[32:35], v[216:219], v[16:19]\n"
   "v_mfma_f32_16x16x32_bf16 v[20:23], a[36:39], v[216:219], v[20:23]\n"
   "v_mfma_f32_16x16x32_bf16 v[24:27], a[40:43], v[216:219], v[24:27]\n"
   "v_mfma_f32_16x16x32_bf16 v[28:31], a[44:47], v[216:219], v[28:31]\n"
   "v_mfma_f32_16x16x32_bf16 v[16:19], a[48:51], v[220:223], v[16:19]\n"
   "v_mfma_f32_16x16x32_bf16 v[20:23], a[52:55], v[220:223], v[20:23]\n"
   "v_mfma_f32_16x16x32_bf16 v[24:27], a[56:59], v[220:223], v[24:27]\n"
   "v_mfma_f32_16x16x32_bf16 v[28:31], a[60:63], v[220:223], v[28:31]\n"
   "v_mfma_f32_16x16x32_bf16 v[16:19], a[64:67], v[232:235], v[16:19]\n"
   "v_mfma_f32_16x16x32_bf16 v[20:23], a[68:71], v[232:235], v[20:23]\n"
   "v_mfma_f32_16x16x32_bf16 v[24:27], a[72:75], v[232:235], v[24:27]\n"
   "v_mfma_f32_16x16x32_bf16 v[28:31], a[76:79], v[232:235], v[28:31]\n"
   "v_mfma_f32_16x16x32_bf16 v[16:19], a[80:83], v[236:239], v[16:19]\n"
   "v_mfma_f32_16x16x32_bf16 v[20:23], a[84:87], v[236:239], v[20:23]\n"
   "v_mfma_f32_16x16x32_bf16 v[24:27], a[88:91], v[236:239], v[24:27]\n"
   "v_mfma_f32_16x16x32_bf16 v[28:31], a[92:95], v[236:239], v[28:31]\n"
   "v_mfma_f32_16x16x32_bf16 v[16:19], a[96:99], v[240:243], v[16:19]\n"
   "v_mfma_f32_16x16x32_bf16 v[20:23], a[100:103], v[240:243], v[20:23]\n"
   "v_mfma_f32_16x16x32_bf16 v[24:27], a[104:107], v[240:243], v[24:27]\n"
   "v_mfma_f32_16x16x32_bf16 v[28:31], a[108:111], v[240:243], v[28:31]\n"
   "s_branch .Lfb_back41\n"
   ".Lfb_stub43:\n"
   "s_waitcnt lgkmcnt(0)\n"
   "buffer_load_dwordx4 v[120:123], v9, s[44:47], s19 offen sc1\n"
   "s_nop 0\n"
   "buffer_load_dwordx4 v[172:175], v9, s[44:47], s19 offen offset:1024 sc1\n"
   "buffer_load_dwordx4 v[188:191], v9, s[44:47], s19 offen offset:2048 sc1\n"
   "buffer_load_dwordx4 v[192:195], v9, s[44:47], s19 offen offset:3072 sc1\n"
   "buffer_load_dwordx4 v[196:199], v6, s[44:47], s19 offen sc1\n"
   "buffer_load_dwordx4 v[208:211], v6, s[44:47], s19 offen offset:1024 sc1\n"
   "buffer_load_dwordx4 v[216:219], v6, s[44:47], s19 offen offset:2048 sc1\n"
   "buffer_load_dwordx4 v[220:223], v6, s[44:47], s19 offen offset:3072 sc1\n"
   "buffer_load_dwordx4 v[232:235], v162, s[44:47], s19 offen sc1\n"
   "buffer_load_dwordx4 v[236:239], v162, s[44:47], s19 offen offset:1024 sc1\n"
   "buffer_load_dwordx4 v[240:243], v163, s[44:47], s19 offen offset:2048 sc1\n"
   "s_waitcnt vmcnt(10)\n"
   "v_xor_b32 v120, v14, v120\n"
   "v_xor_b32 v121, v15, v121\n"
   "v_xor_b32 v122, v114, v122\n"
   "v_xor_b32_e64 v123, v115, v123\n"
   "v_or3_b32 v2, v120, v121, v121\n"
   "v_or3_b32 v2, v122, v123, v2\n"
   "s_waitcnt vmcnt(9)\n"
   "v_xor_b32 v172, v14, v172\n"
   "v_xor_b32 v173, v15, v173\n"
   "v_xor_b32 v174, v114, v174\n"
   "v_xor_b32_e64 v175, v115, v175\n"
   "v_or3_b32 v2, v172, v173, v2\n"
   "v_or3_b32 v2, v174, v175, v2\n"
   "s_waitcnt vmcnt(8)\n"
   "v_xor_b32 v188, v14, v188\n"
   "v_xor_b32 v189, v15, v189\n"
   "v_xor_b32 v190, v114, v190\n"
   "v_xor_b32_e64 v191, v115, v191\n"
   "v_or3_b32 v2, v188, v189, v2\n"
   "v_or3_b32 v2, v190, v191, v2\n"
   "s_waitcnt vmcnt(7)\n"
   "v_xor_b32 v192, v14, v192\n"
   "v_xor_b32 v193, v15, v193\n"
   "v_xor_b32 v194, v114, v194\n"
   "v_xor_b32_e64 v195, v115, v195\n"
   "v_or3_b32 v2, v192, v193, v2\n"
   "v_or3_b32 v2, v194, v195, v2\n"
   "s_waitcnt vmcnt(6)\n"
   "v_xor_b32 v196, v14, v196\n"
   "v_xor_b32 v197, v15, v197\n"
   "v_xor_b32 v198, v114, v198\n"
   "v_xor_b32_e64 v199, v115, v199\n"
   "v_or3_b32 v2, v196, v197, v2\n"
   "v_or3_b32 v2, v198, v199, v2\n"
   "s_waitcnt vmcnt(5)\n"
   "v_xor_b32 v208, v14, v208\n"
   "v_xor_b32 v209, v15, v209\n"
   "v_xor_b32 v210, v114, v210\n"
   "v_xor_b32_e64 v211, v115, v211\n"
   "v_or3_b32 v2, v208, v209, v2\n"
   "v_or3_b32 v2, v210, v211, v2\n"
   "s_waitcnt vmcnt(4)\n"
   "v_xor_b32 v216, v14, v216\n"
   "v_xor_b32 v217, v15, v217\n"
   "v_xor_b32 v218, v114, v218\n"
   "v_xor_b32_e64 v219, v115, v219\n"
   "v_or3_b32 v2, v216, v217, v2\n"
   "v_or3_b32 v2, v218, v219, v2\n"
   "s_waitcnt vmcnt(3)\n"
   "v_xor_b32 v220, v14, v220\n"
   "v_xor_b32 v221, v15, v221\n"
   "v_xor_b32 v222, v114, v222\n"
   "v_xor_b32_e64 v223, v115, v223\n"
   "v_or3_b32 v2, v220, v221, v2\n"
   "v_or3_b32 v2, v222, v223, v2\n"
   "s_waitcnt vmcnt(2)\n"
   "v_xor_b32 v232, v14, v232\n"
   "v_xor_b32 v233, v15, v233\n"
   "v_xor_b32 v234, v114, v234\n"
   "v_xor_b32_e64 v235, v115, v235\n"
   "v_or3_b32 v2, v232, v233, v2\n"
   "v_or3_b32 v2, v234, v235, v2\n"
   "s_waitcnt vmcnt(1)\n"
   "v_xor_b32 v236, v14, v236\n"
   "v_xor_b32 v237, v15, v237\n"
   "v_xor_b32 v238, v114, v238\n"
   "v_xor_b32_e64 v239, v115, v239\n"
   "v_or3_b32 v2, v236, v237, v2\n"
   "v_or3_b32 v2, v238, v239, v2\n"
   "s_waitcnt vmcnt(0)\n"
   "v_xor_b32 v240, v14, v240\n"
   "v_xor_b32 v241, v15, v241\n"
   "v_xor_b32 v242, v114, v242\n"
   "v_xor_b32 v243, v115, v243\n"
   "v_or3_b32 v2, v240, v241, v2\n"
   "v_or3_b32 v2, v242, v243, v2\n"
   "v_and_b32 v2, 0x80008000, v2\n"
   "v_cmp_ne_u32 vcc, 0, v2\n"
   "s_waitcnt vmcnt(0) lgkmcnt(0)\n"
   "s_cbranch_vccz .Lfb_aft45\n"
   "s_add_u32 s50, s50, 1\n"
   "s_cmp_lt_u32 s50, 0x30d40\n"
   "s_cbranch_scc1 .Lfb_stub43\n"
   ".Lfb_aft45:\n"
   "v_mfma_f32_16x16x32_bf16 v[32:35], a[192:195], v[120:123], 0\n"
   "v_mfma_f32_16x16x32_bf16 v[36:39], a[196:199], v[120:123], 0\n"
   "v_mfma_f32_16x16x32_bf16 v[40:43], a[200:203], v[120:123], 0\n"
   "v_mfma_f32_16x16x32_bf16 v[44:47], a[204:207], v[120:123], 0\n"
   "v_mfma_f32_16x16x32_bf16 v[32:35], a[208:211], v[172:175], v[32:35]\n"
   "v_mfma_f32_16x16x32_bf16 v[36:39], a[212:215], v[172:175], v[36:39]\n"
   "v_mfma_f32_16x16x32_bf16 v[40:43], a[216:219], v[172:175], v[40:43]\n"
   "v_mfma_f32_16x16x32_bf16 v[44:47], a[220:223], v[172:175], v[44:47]\n"
   "v_mfma_f32_16x16x32_bf16 v[32:35], a[224:227], v[188:191], v[32:35]\n"
   "v_mfma_f32_16x16x32_bf16 v[36:39], a[228:231], v[188:191], v[36:39]\n"
   "v_mfma_f32_16x16x32_bf16 v[40:43], a[232:235], v[188:191], v[40:43]\n"
   "v_mfma_f32_16x16x32_bf16 v[44:47], a[236:239], v[188:191], v[44:47]\n"
   "v_mfma_f32_16x16x32_bf16 v[32:35], a[240:243], v[192:195], v[32:35]\n"
   "v_mfma_f32_16x16x32_bf16 v[36:39], a[244:247], v[192:195], v[36:39]\n"
   "v_mfma_f32_16x16x32_bf16 v[40:43], a[248:251], v[192:195], v[40:43]\n"
   "v_mfma_f32_16x16x32_bf16 v[44:47], a[252:255], v[192:195], v[44:47]\n"
   "v_mfma_f32_16x16x32_bf16 v[32:35], a[0:3], v[196:199], v[32:35]\n"
   "v_mfma_f32_16x16x32_bf16 v[36:39], a[4:7], v[196:199], v[36:39]\n"
   "v_mfma_f32_16x16x32_bf16 v[40:43], a[8:11], v[196:199], v[40:43]\n"
   "v_mfma_f32_16x16x32_bf16 v[44:47], a[12:15], v[196:199], v[44:47]\n"
   "v_mfma_f32_16x16x32_bf16 v[32:35], a[16:19], v[208:211], v[32:35]\n"
   "v_mfma_f32_16x16x32_bf16 v[36:39], a[20:23], v[208:211], v[36:39]\n"
   "v_mfma_f32_16x16x32_bf16 v[40:43], a[24:27], v[208:211], v[40:43]\n"
   "v_mfma_f32_16x16x32_bf16 v[44:47], a[28:31], v[208:211], v[44:47]\n"
   "v_mfma_f32_16x16x32_bf16 v[32:35], a[32:35], v[216:219], v[32:35]\n"
   "v_mfma_f32_16x16x32_bf16 v[36:39], a[36:39], v[216:219], v[36:39]\n"
   "v_mfma_f32_16x16x32_bf16 v[40:43], a[40:43], v[216:219], v[40:43]\n"
   "v_mfma_f32_16x16x32_bf16 v[44:47], a[44:47], v[216:219], v[44:47]\n"
   "v_mfma_f32_16x16x32_bf16 v[32:35], a[48:51], v[220:223], v[32:35]\n"
   "v_mfma_f32_16x16x32_bf16 v[36:39], a[52:55], v[220:223], v[36:39]\n"
   "v_mfma_f32_16x16x32_bf16 v[40:43], a[56:59], v[220:223], v[40:43]\n"
   "v_mfma_f32_16x16x32_bf16 v[44:47], a[60:63], v[220:223], v[44:47]\n"
   "v_mfma_f32_16x16x32_bf16 v[32:35], a[64:67], v[232:235], v[32:35]\n"
   "v_mfma_f32_16x16x32_bf16 v[36:39], a[68:71], v[232:235], v[36:39]\n"
   "v_mfma_f32_16x16x32_bf16 v[40:43], a[72:75], v[232:235], v[40:43]\n"
   "v_mfma_f32_16x16x32_bf16 v[44:47], a[76:79], v[232:235], v[44:47]\n"
   "v_mfma_f32_16x16x32_bf16 v[32:35], a[80:83], v[236:239], v[32:35]\n"
   "v_mfma_f32_16x16x32_bf16 v[36:39], a[84:87], v[236:239], v[36:39]\n"
   "v_mfma_f32_16x16x32_bf16 v[40:43], a[88:91], v[236:239], v[40:43]\n"
   "v_mfma_f32_16x16x32_bf16 v[44:47], a[92:95], v[236:239], v[44:47]\n"
   "v_mfma_f32_16x16x32_bf16 v[32:35], a[96:99], v[240:243], v[32:35]\n"
   "v_mfma_f32_16x16x32_bf16 v[36:39], a[100:103], v[240:243], v[36:39]\n"
   "v_mfma_f32_16x16x32_bf16 v[40:43], a[104:107], v[240:243], v[40:43]\n"
   "v_mfma_f32_16x16x32_bf16 v[44:47], a[108:111], v[240:243], v[44:47]\n"
   "s_branch .Lfb_back44\n"
   :: "{s[0:1]}"(__builtin_amdgcn_kernarg_segment_ptr()), "{s2}"(__builtin_amdgcn_workgroup_id_x()),
      "{v0}"(__builtin_amdgcn_workitem_id_x()),
      "{s3}"((uint32_t)(uintptr_t)(__attribute__((address_space(3))) char*)lds)
   : "memory", "v255", "a255", "s99", "vcc");
#endif
}

extern "C" void kernel_launch(void* const* d_in, const int* in_sizes, int n_in, void* d_out, int out_size,
                              void* d_ws, size_t ws_size, hipStream_t stream) {
  (void)n_in; (void)ws_size;
  Args a;
  const int idx[8] = {0, 1, 2, 3, 5, 6, 7, 8};
  for (int i = 0; i < 8; ++i) { a.s[i].p = d_in[idx[i]]; a.s[i].n = 2u * (uint32_t)in_sizes[idx[i]]; a.s[i].f = 0x20000u; }
  a.s[8].p = d_out; a.s[8].n = 2u * (uint32_t)out_size; a.s[8].f = 0x20000u;
  a.s[9].p = d_ws;  a.s[9].n = 17825792u; a.s[9].f = 0x20000u;
  fused_block<<<256, 256, 0, stream>>>(a);
}
```
